```python
import torch

def _softmax(x, axis=-1):
    return torch.softmax(x, dim=axis)
def _gelu(x):
    return torch.nn.functional.gelu(x, approximate="tanh")
def _silu(x):
    return torch.nn.functional.silu(x)
def _sigmoid(x):
    return torch.sigmoid(x)
def _layernorm(x, axis=-1, eps=1e-5):
    m = x.mean(dim=axis, keepdim=True)
    v = x.var(dim=axis, keepdim=True, unbiased=False)
    return (x - m) / torch.sqrt(v + eps)
def _scatter_add(tgt, idx, src, axis=0):
    return tgt.clone().index_add_(axis, idx, src)
def _segment_sum(src, seg, n):
    out = torch.zeros((n,) + src.shape[1:], dtype=src.dtype)
    return out.index_add_(0, seg, src)
def _pad(x, pw):
    flat = tuple(v for pair in reversed(pw) for v in pair)
    return torch.nn.functional.pad(x, flat)

kernel_name = 'standalone__weight_only_int_quip__prompt'

def setup_inputs():

    _w = (lambda a: (lambda t: t.to(torch.bfloat16) if t.is_floating_point() else t)(torch.as_tensor(a)))
    B, S, D, F = 1, 2048, 640, 1536
    G = -1 if -1 > 0 else D
    D = (D // G) * G
    ng = D // G
    bits = 8 if 8 > 0 else 4
    qmax = 1 << bits
    qw = torch.randint(0, qmax, (D, F)).to(torch.int32)
    U, _ = (lambda *_a, **_k: torch.linalg.qr(*[torch.as_tensor(_x).float() for _x in _a], **_k))((lambda *s: torch.randn(*s, dtype=torch.bfloat16) * 0.05)(D, D))
    V, _ = (lambda *_a, **_k: torch.linalg.qr(*[torch.as_tensor(_x).float() for _x in _a], **_k))((lambda *s: torch.randn(*s, dtype=torch.bfloat16) * 0.05)(F, F))
    return {
        "x": _w((lambda *s: torch.randn(*s, dtype=torch.bfloat16) * 0.05)(B, S, D)),
        "qw": _w(qw),
        "scales": _w(torch.abs((lambda *s: torch.randn(*s, dtype=torch.bfloat16) * 0.05)(ng, F)) * 0.02 + 0.005),
        "zeros": _w(torch.full((ng, F), qmax / 2.0, dtype=float)),
        "U": _w(U.to(float)),
        "V": _w(V.to(float)),
    }

def reference(x, qw, scales, zeros, U, V):
    B, S, D = x.shape
    ng, F = scales.shape
    G = D // ng
    s = (lambda x, n, axis: torch.repeat_interleave(x, n, dim=axis))(scales, G, axis=0)
    z = (lambda x, n, axis: torch.repeat_interleave(x, n, dim=axis))(zeros, G, axis=0)
    wq = ((lambda x, dt: x.to(dt))(qw, x.dtype) - z) * s
    xr = torch.einsum("bsd,de->bse", x, U)
    h  = torch.einsum("bse,ef->bsf", xr, wq)
    y  = torch.einsum("bsf,fg->bsg", h, V)
    return y
```

```cpp
#include <hip/hip_runtime.h>
#include <hip/hip_bf16.h>
#include <stdint.h>

using bf16x8 = __attribute__((ext_vector_type(8))) short;
using f32x4 = __attribute__((ext_vector_type(4))) float;

constexpr int S_ = 2048, D_ = 640, F_ = 1536;

__device__ __forceinline__ float bf2f(unsigned short u) { return __uint_as_float(((unsigned)u) << 16); }
__device__ __forceinline__ unsigned short f2bf(float f) {
    __hip_bfloat16 b = __float2bfloat16(f);
    return *reinterpret_cast<unsigned short*>(&b);
}

template <int MODE>
__global__ void gemm_naive(const void* Av, const unsigned short* B, unsigned short* C, int M, int N, int K,
                           const unsigned short* scales, const unsigned short* zeros) {
    int wave = (blockIdx.x * blockDim.x + threadIdx.x) >> 6;
    int lane = threadIdx.x & 63;
    int tn = N / 16;
    int tm_i = wave / tn, tn_i = wave % tn;
    if (tm_i * 16 >= M) return;
    int r = lane & 15, g = lane >> 4;
    f32x4 acc = {0, 0, 0, 0};
    for (int k0 = 0; k0 < K; k0 += 32) {
        bf16x8 a, b;
        for (int j = 0; j < 8; ++j) {
            int k = k0 + g * 8 + j;
            if (MODE == 0) {
                a[j] = ((const unsigned short*)Av)[(size_t)(tm_i * 16 + r) * K + k];
            } else {
                int q = ((const int*)Av)[(size_t)(tm_i * 16 + r) * K + k];
                float w = ((float)q - bf2f(zeros[k])) * bf2f(scales[k]);
                a[j] = f2bf(w);
            }
            b[j] = B[(size_t)k * N + tn_i * 16 + r];
        }
        acc = __builtin_amdgcn_mfma_f32_16x16x32_bf16(a, b, acc, 0, 0, 0);
    }
    for (int j = 0; j < 4; ++j) C[(size_t)(tm_i * 16 + g * 4 + j) * N + tn_i * 16 + r] = f2bf(acc[j]);
}

extern "C" void kernel_launch(void* const* d_in, const int* in_sizes, int n_in, void* d_out, int out_size,
                              void* d_ws, size_t ws_size, hipStream_t stream) {
    const unsigned short* x = (const unsigned short*)d_in[0];
    const int* qw = (const int*)d_in[1];
    const unsigned short* scales = (const unsigned short*)d_in[2];
    const unsigned short* zeros = (const unsigned short*)d_in[3];
    const unsigned short* U = (const unsigned short*)d_in[4];
    const unsigned short* V = (const unsigned short*)d_in[5];
    unsigned short* xr = (unsigned short*)d_ws;
    unsigned short* Mm = xr + (size_t)S_ * D_;
    unsigned short* y = (unsigned short*)d_out;
    gemm_naive<0><<<(S_ / 16) * (D_ / 16) / 4, 256, 0, stream>>>(x, U, xr, S_, D_, D_, nullptr, nullptr);
    gemm_naive<1><<<(D_ / 16) * (F_ / 16) / 4, 256, 0, stream>>>(qw, V, Mm, D_, F_, F_, scales, zeros);
    gemm_naive<0><<<(S_ / 16) * (F_ / 16) / 4, 256, 0, stream>>>(xr, Mm, y, S_, F_, D_, nullptr, nullptr);
}
```

```cpp
#include <hip/hip_runtime.h>
#include <stdint.h>
#pragma clang diagnostic ignored "-Wint-to-pointer-cast"

#define FUSED 1

typedef unsigned u32;
typedef unsigned long long u64;
using bf16x8 = __attribute__((ext_vector_type(8))) short;
using s16x4 = __attribute__((ext_vector_type(4))) short;
using f32x4 = __attribute__((ext_vector_type(4))) float;
using u32x4 = __attribute__((ext_vector_type(4))) unsigned;
using u32x3 = __attribute__((ext_vector_type(3))) unsigned;
using u32x2 = __attribute__((ext_vector_type(2))) unsigned;
using bf16x2_t = __attribute__((ext_vector_type(2))) __bf16;
using f32x2 = __attribute__((ext_vector_type(2))) float;
#define LAS __attribute__((address_space(3)))
typedef __amdgpu_buffer_rsrc_t rsrc_t;

constexpr int S_ = 2048, D_ = 640, F_ = 1536;
constexpr u32 XF_OFF = 0, XF_BYTES = S_ * D_ * 2;
constexpr u32 MF_OFF = XF_BYTES, MF_BYTES = 2 * D_ * F_ * 2;
constexpr u32 FL_OFF = MF_OFF + MF_BYTES;
constexpr u32 EP_OFF = FL_OFF + 4096;
constexpr u32 WS_BYTES = EP_OFF + 256;
constexpr int LDS_BYTES = 163840;
constexpr u64 TAGC = 0x9E3779B97F4A7C15ull;
constexpr int AUX_HO = FUSED ? 16 : 0;

struct Params {
    const void *x, *qw, *sc, *ze, *U, *V;
    void *y, *ws;
};

__device__ __forceinline__ rsrc_t mk(const void* p, u32 bytes) { return __builtin_amdgcn_make_buffer_rsrc((void*)p, (short)0, (int)bytes, 0x00020000); }
__device__ __forceinline__ void glds(u32 m0v, u32 voff, rsrc_t r, u32 soff) {
    asm volatile("s_mov_b32 m0, %0\n\ts_nop 0\n\tbuffer_load_dwordx4 %1, %2, %3 offen lds" ::"s"(m0v), "v"(voff), "s"(r), "s"(soff) : "memory");
}
__device__ __forceinline__ void gld(u32x4& d, u32 voff, rsrc_t r, u32 soff) {
    asm volatile("buffer_load_dwordx4 %0, %1, %2, %3 offen" : "=v"(d) : "v"(voff), "s"(r), "s"(soff) : "memory");
}
__device__ __forceinline__ void gld_ho(u32x4& d, u32 voff, rsrc_t r, u32 soff) {
    asm volatile("buffer_load_dwordx4 %0, %1, %2, %3 offen sc1" : "=v"(d) : "v"(voff), "s"(r), "s"(soff) : "memory");
}
template <int N> __device__ __forceinline__ void waitv() { asm volatile("s_waitcnt vmcnt(%0)" ::"n"(N) : "memory"); }
template <int N> __device__ __forceinline__ void waitv4(u32x4& a, u32x4& b, u32x4& c, u32x4& d) {
    asm volatile("s_waitcnt vmcnt(%4)" : "+v"(a), "+v"(b), "+v"(c), "+v"(d) : "n"(N) : "memory");
}
template <int N> __device__ __forceinline__ void waitv6(u32x4& a, u32x4& b, u32x4& c, u32x4& d, u32x4& e, u32x4& f) {
    asm volatile("s_waitcnt vmcnt(%6)" : "+v"(a), "+v"(b), "+v"(c), "+v"(d), "+v"(e), "+v"(f) : "n"(N) : "memory");
}
__device__ __forceinline__ void bar() { asm volatile("s_waitcnt lgkmcnt(0)\n\ts_barrier" ::: "memory"); }
__device__ __forceinline__ u32 pk(float a, float b) {
    f32x2 v = {a, b};
    return __builtin_bit_cast(u32, __builtin_convertvector(v, bf16x2_t));
}
__device__ __forceinline__ bf16x8 ldsr128(u32 a) { return *(const LAS bf16x8*)a; }
__device__ __forceinline__ s16x4 ldstr(u32 a) { return __builtin_amdgcn_ds_read_tr16_b64_v4i16((LAS s16x4*)a); }
__device__ __forceinline__ bf16x8 cat(s16x4 a, s16x4 b) { return __builtin_shufflevector(a, b, 0, 1, 2, 3, 4, 5, 6, 7); }
#define MFMA(a, b, c) __builtin_amdgcn_mfma_f32_16x16x32_bf16(a, b, c, 0, 0, 0)

__shared__ __attribute__((aligned(1024))) char smem[LDS_BYTES];

constexpr u32 MB_SLOT = 24576, MA_OFF = 5 * MB_SLOT, MS_OFF = MA_OFF + 2 * 8192, MZ_OFF = MS_OFF + 3072;

template <int ST> struct MProd {
    static __device__ __forceinline__ void issue(u32x4 (&ra)[4][4], u32 voffA, const u32 (&voffB)[6], rsrc_t rq, rsrc_t rv, u32 ldsb) {
#pragma unroll
        for (int i = 0; i < 4; ++i) gld(ra[ST & 3][i], voffA, rq, ST * 256 + i * 24576);
#pragma unroll
        for (int j = 0; j < 6; ++j) glds(ldsb + (ST % 5) * MB_SLOT + j * 1024, voffB[j], rv, ST * 196608);
    }
    static __device__ __forceinline__ void step(u32x4 (&ra)[4][4], u32 voffA, const u32 (&voffB)[6], const u32 (&wa)[4], u32 sca, rsrc_t rq, rsrc_t rv, u32 ldsb) {
        constexpr int REM = (11 - ST) < 3 ? (11 - ST) : 3;
        const f32x4 s4 = *(const LAS f32x4*)(sca + MS_OFF + ST * 256);
        const f32x4 z4 = *(const LAS f32x4*)(sca + MZ_OFF + ST * 256);
        waitv4<REM * 10>(ra[ST & 3][0], ra[ST & 3][1], ra[ST & 3][2], ra[ST & 3][3]);
#pragma unroll
        for (int i = 0; i < 4; ++i) {
            const u32x4 q = ra[ST & 3][i];
            u32x2 o;
            o[0] = pk(__builtin_fmaf((float)(int)q[0], s4[0], z4[0]), __builtin_fmaf((float)(int)q[1], s4[1], z4[1]));
            o[1] = pk(__builtin_fmaf((float)(int)q[2], s4[2], z4[2]), __builtin_fmaf((float)(int)q[3], s4[3], z4[3]));
            *(LAS u32x2*)(wa[i] + (ST & 1) * 8192) = o;
        }
        bar();
        if constexpr (ST + 4 < 12) MProd<ST + 4>::issue(ra, voffA, voffB, rq, rv, ldsb);
        if constexpr (ST + 1 < 12) MProd<ST + 1>::step(ra, voffA, voffB, wa, sca, rq, rv, ldsb);
    }
};

__device__ __forceinline__ void m_role(const Params& p, int wave, int lane, int et, int gt, int half) {
    const u32 base = (u32)(uintptr_t)smem;
    const int q = lane >> 4, c = lane & 15;
    const int kb = half * 768;
    if (wave >= 4) {
        const int pw = wave - 4;
        const rsrc_t rq = mk(p.qw, D_ * F_ * 4), rv = mk(p.V, F_ * F_ * 2);
        const u32 voffA = ((et * 64 + 16 * pw + q) * F_ + kb + 4 * c) * 4;
        u32 voffB[6], wa[4];
#pragma unroll
        for (int j = 0; j < 6; ++j) {
            const int P = (6 * pw + j) * 1024 + 16 * lane;
            const int r = P / 384, o = P - r * 384;
            const int sw = ((r >> 1) & 1) | (((r >> 3) & 1) << 1);
            voffB[j] = (kb + r) * (F_ * 2) + gt * 384 + (((o >> 5) ^ sw) << 5) + (o & 16);
        }
#pragma unroll
        for (int i = 0; i < 4; ++i) {
            const int row = 16 * pw + 4 * i + q;
            wa[i] = base + MA_OFF + row * 128 + (((c >> 1) ^ ((row >> 1) & 7)) << 4) + (c & 1) * 8;
        }
        const u32 ldsb = base + pw * 6144;
        u32x4 ra[4][4];
        MProd<0>::issue(ra, voffA, voffB, rq, rv, ldsb);
        MProd<1>::issue(ra, voffA, voffB, rq, rv, ldsb);
        MProd<2>::issue(ra, voffA, voffB, rq, rv, ldsb);
        MProd<3>::issue(ra, voffA, voffB, rq, rv, ldsb);
        bar();
        MProd<0>::step(ra, voffA, voffB, wa, base + c * 16, rq, rv, ldsb);
    } else {
        {
            const int t = wave * 64 + lane;
            if (t < 192) {
                const u32x2 sv = *(const u32x2*)((const char*)p.sc + (kb + 4 * t) * 2);
                const u32x2 zv = *(const u32x2*)((const char*)p.ze + (kb + 4 * t) * 2);
                f32x4 s4, z4;
                s4[0] = __uint_as_float(sv[0] << 16); s4[1] = __uint_as_float(sv[0] & 0xffff0000u);
                s4[2] = __uint_as_float(sv[1] << 16); s4[3] = __uint_as_float(sv[1] & 0xffff0000u);
                z4[0] = -__uint_as_float(zv[0] << 16) * s4[0]; z4[1] = -__uint_as_float(zv[0] & 0xffff0000u) * s4[1];
                z4[2] = -__uint_as_float(zv[1] << 16) * s4[2]; z4[3] = -__uint_as_float(zv[1] & 0xffff0000u) * s4[3];
                *(LAS f32x4*)(base + MS_OFF + t * 16) = s4;
                *(LAS f32x4*)(base + MZ_OFF + t * 16) = z4;
            }
        }
        bar();
        const int qq = c >> 2, pp = c & 3;
        const int sw = (qq >> 1) | ((q & 1) << 1);
        u32 ab[2], bb[3];
        ab[0] = base + MA_OFF + c * 128 + ((q ^ ((c >> 1) & 7)) << 4);
        ab[1] = ab[0] ^ 64;
#pragma unroll
        for (int n = 0; n < 3; ++n) bb[n] = base + (8 * q + qq) * 384 + (((3 * wave + n) ^ sw) << 5) + 8 * pp;
        f32x4 acc[4][3] = {};
#pragma unroll
        for (int st = 0; st < 12; ++st) {
            bar();
#pragma unroll
            for (int ks = 0; ks < 2; ++ks) {
                bf16x8 a[4], b[3];
#pragma unroll
                for (int n = 0; n < 3; ++n) {
                    const u32 ad = bb[n] + (st % 5) * MB_SLOT + ks * 12288;
                    b[n] = cat(ldstr(ad), ldstr(ad + 1536));
                }
#pragma unroll
                for (int t = 0; t < 4; ++t) a[t] = ldsr128(ab[ks] + (st & 1) * 8192 + t * 2048);
#pragma unroll
                for (int t = 0; t < 4; ++t)
#pragma unroll
                    for (int n = 0; n < 3; ++n) acc[t][n] = MFMA(a[t], b[n], acc[t][n]);
            }
        }
        const rsrc_t rw = mk(p.ws, WS_BYTES);
#pragma unroll
        for (int ebl = 0; ebl < 2; ++ebl)
#pragma unroll
            for (int n = 0; n < 3; ++n) {
                const int nt = 3 * wave + n;
                const int gb = 2 * gt + nt / 6, np = nt % 6;
                u32x4 v;
                v[0] = pk(acc[2 * ebl][n][0], acc[2 * ebl][n][1]);
                v[1] = pk(acc[2 * ebl][n][2], acc[2 * ebl][n][3]);
                v[2] = pk(acc[2 * ebl + 1][n][0], acc[2 * ebl + 1][n][1]);
                v[3] = pk(acc[2 * ebl + 1][n][2], acc[2 * ebl + 1][n][3]);
                const u32 off = MF_OFF + (((half * 16 + gb) * 20 + 2 * et + ebl) * 6 + np) * 1024;
                __builtin_amdgcn_raw_buffer_store_b128(v, rw, lane * 16, off, AUX_HO);
            }
    }
}

constexpr u32 XS_SLOT = 32768;
template <int ST> struct XProd {
    static __device__ __forceinline__ void issue(const u32 (&vU)[2], const u32 (&vX)[2], rsrc_t ru, rsrc_t rx, u32 ldsb) {
#pragma unroll
        for (int j = 0; j < 4; ++j) glds(ldsb + (ST % 5) * XS_SLOT + 16384 + j * 1024, vX[j & 1], rx, ST * 128 + j * 8 * 1280);
#pragma unroll
        for (int j = 0; j < 4; ++j) glds(ldsb + (ST % 5) * XS_SLOT + j * 1024, vU[j >> 1], ru, ST * 64 * 1280 + j * 4 * 1280);
    }
    static __device__ __forceinline__ void step(const u32 (&vU)[2], const u32 (&vX)[2], rsrc_t ru, rsrc_t rx, u32 ldsb) {
        constexpr int REM = (9 - ST) < 3 ? (9 - ST) : 3;
        waitv<REM * 8>();
        bar();
        if constexpr (ST + 4 < 10) XProd<ST + 4>::issue(vU, vX, ru, rx, ldsb);
        if constexpr (ST + 1 < 10) XProd<ST + 1>::step(vU, vX, ru, rx, ldsb);
    }
};

__device__ __forceinline__ void x_role(const Params& p, int wave, int lane, int sb, int eblk) {
    const u32 base = (u32)(uintptr_t)smem;
    const int q = lane >> 4, c = lane & 15;
    if (wave >= 4) {
        const int pw = wave - 4;
        const rsrc_t ru = mk(p.U, D_ * D_ * 2), rx = mk(p.x, S_ * D_ * 2);
        u32 vU[2], vX[2];
#pragma unroll
        for (int h = 0; h < 2; ++h) {
            vU[h] = (16 * pw + q) * 1280 + eblk * 256 + (((c >> 1) ^ (q | (h << 2))) << 5) + (c & 1) * 16;
            vX[h] = (sb * 128 + 32 * pw + (lane >> 3)) * 1280 + (((lane & 7) ^ ((4 * h + q) & 7)) << 4);
        }
        const u32 ldsb = base + pw * 4096;
        XProd<0>::issue(vU, vX, ru, rx, ldsb);
        XProd<1>::issue(vU, vX, ru, rx, ldsb);
        XProd<2>::issue(vU, vX, ru, rx, ldsb);
        XProd<3>::issue(vU, vX, ru, rx, ldsb);
        XProd<0>::step(vU, vX, ru, rx, ldsb);
    } else {
        const int we = wave & 1, ws = wave >> 1;
        const int qq = c >> 2, pp = c & 3;
        const int sw = qq | ((q & 1) << 2);
        u32 ab[4], bb[2];
#pragma unroll
        for (int t = 0; t < 4; ++t) ab[t] = base + (8 * q + qq) * 256 + (((4 * we + t) ^ sw) << 5) + 8 * pp;
        bb[0] = base + 16384 + (64 * ws + c) * 128 + ((q ^ ((c >> 1) & 7)) << 4);
        bb[1] = bb[0] ^ 64;
        f32x4 acc[4][4] = {};
#pragma unroll
        for (int st = 0; st < 10; ++st) {
            bar();
#pragma unroll
            for (int ks = 0; ks < 2; ++ks) {
                bf16x8 a[4], b[4];
#pragma unroll
                for (int t = 0; t < 4; ++t) {
                    const u32 ad = ab[t] + (st % 5) * XS_SLOT + ks * 8192;
                    a[t] = cat(ldstr(ad), ldstr(ad + 1024));
                }
#pragma unroll
                for (int u = 0; u < 4; ++u) b[u] = ldsr128(bb[ks] + (st % 5) * XS_SLOT + u * 2048);
#pragma unroll
                for (int t = 0; t < 4; ++t)
#pragma unroll
                    for (int u = 0; u < 4; ++u) acc[t][u] = MFMA(a[t], b[u], acc[t][u]);
            }
        }
        const rsrc_t rw = mk(p.ws, WS_BYTES);
#pragma unroll
        for (int ebl = 0; ebl < 2; ++ebl)
#pragma unroll
            for (int u = 0; u < 4; ++u) {
                u32x4 v;
                v[0] = pk(acc[2 * ebl][u][0], acc[2 * ebl][u][1]);
                v[1] = pk(acc[2 * ebl][u][2], acc[2 * ebl][u][3]);
                v[2] = pk(acc[2 * ebl + 1][u][0], acc[2 * ebl + 1][u][1]);
                v[3] = pk(acc[2 * ebl + 1][u][2], acc[2 * ebl + 1][u][3]);
                const u32 off = XF_OFF + ((sb * 20 + 4 * eblk + 2 * we + ebl) * 8 + 4 * ws + u) * 1024;
                __builtin_amdgcn_raw_buffer_store_b128(v, rw, lane * 16, off, AUX_HO);
            }
    }
}

constexpr u32 YQ = 1552, YEB = 4 * YQ;
constexpr int YD = 5;
__device__ __forceinline__ u32 add2(u32 a, u32 b) {
    const float lo = __uint_as_float(a << 16) + __uint_as_float(b << 16);
    const float hi = __uint_as_float(a & 0xffff0000u) + __uint_as_float(b & 0xffff0000u);
    return pk(lo, hi);
}
template <int ST> struct YProd {
    static __device__ __forceinline__ void issue(u32x4 (&r)[YD][6], u32 voff, rsrc_t rw) {
#pragma unroll
        for (int j = 0; j < 3; ++j) {
            gld_ho(r[ST % YD][2 * j], voff, rw, (ST * 12 + j) * 1024);
            gld_ho(r[ST % YD][2 * j + 1], voff, rw, (ST * 12 + j) * 1024 + 16 * 120 * 1024);
        }
    }
    static __device__ __forceinline__ void step(u32x4 (&r)[YD][6], u32 voff, const u32 (&wa)[3], rsrc_t rw) {
        constexpr int REM = (9 - ST) < (YD - 1) ? (9 - ST) : (YD - 1);
        u32x4(&x)[6] = r[ST % YD];
        waitv6<REM * 6>(x[0], x[1], x[2], x[3], x[4], x[5]);
#pragma unroll
        for (int j = 0; j < 3; ++j) {
            u32x4 o;
#pragma unroll
            for (int e = 0; e < 4; ++e) o[e] = add2(x[2 * j][e], x[2 * j + 1][e]);
            *(LAS u32x4*)(wa[j] + ST * 2 * YEB) = o;
        }
        if constexpr (ST + YD < 10) YProd<ST + YD>::issue(r, voff, rw);
        bar();
        if constexpr (ST + 1 < 10) YProd<ST + 1>::step(r, voff, wa, rw);
    }
};

__device__ __forceinline__ bool poll(const u64* f, bool active, u64 tag) {
    for (u32 spins = 0; spins < (1u << 16); ++spins) {
        u64 v = tag;
        if (active) v = __hip_atomic_load(f, __ATOMIC_RELAXED, __HIP_MEMORY_SCOPE_AGENT);
        if (__all(v == tag)) return true;
        __builtin_amdgcn_s_sleep(1);
    }
    return false;
}

__device__ __forceinline__ void y_role(const Params& p, int wave, int lane, int sb, int gb, u64 tag, bool updater) {
    const u32 base = (u32)(uintptr_t)smem;
    const int q = lane >> 4, c = lane & 15;
    const rsrc_t rw = mk(p.ws, WS_BYTES);
    if (wave >= 4) {
        const int pw = wave - 4;
        const u32 voff = MF_OFF + (gb * 120 + 3 * pw) * 1024 + lane * 16;
        u32 wa[3];
#pragma unroll
        for (int j = 0; j < 3; ++j) {
            const int ci = 3 * pw + j;
            wa[j] = base + (ci / 6) * YEB + q * YQ + (16 * (ci % 6) + c) * 16;
        }
        const u64* fl = (const u64*)((const char*)p.ws + FL_OFF);
        if (pw == 0) poll(fl + 256 + sb * 8 + lane, lane < 5, tag);
        bar();
        if (pw == 0) poll(fl + (gb >> 1) * 32 + lane, lane < 20, tag);
        if (pw == 1 && updater) {
#pragma unroll 1
            for (int i = 0; i < 4; ++i) poll(fl + i * 64 + lane, (lane & 31) < 20, tag);
#pragma unroll 1
            for (int i = 0; i < 2; ++i) poll(fl + 256 + i * 64 + lane, (lane & 7) < 5, tag);
            poll(fl + 384 + lane, lane < 16, tag);
            if (lane == 0) __hip_atomic_store((u64*)((char*)p.ws + EP_OFF), tag, __ATOMIC_RELAXED, __HIP_MEMORY_SCOPE_AGENT);
        }
        waitv<0>();
        bar();
        u32x4 r[YD][6];
        YProd<0>::issue(r, voff, rw);
        YProd<1>::issue(r, voff, rw);
        YProd<2>::issue(r, voff, rw);
        YProd<3>::issue(r, voff, rw);
        YProd<4>::issue(r, voff, rw);
        YProd<0>::step(r, voff, wa, rw);
    } else {
        bar();
        bf16x8 xa[20][2];
#pragma unroll
        for (int eb = 0; eb < 20; ++eb)
#pragma unroll
            for (int u = 0; u < 2; ++u)
                xa[eb][u] = __builtin_bit_cast(bf16x8, __builtin_amdgcn_raw_buffer_load_b128(rw, XF_OFF + (sb * 160 + 2 * wave) * 1024 + lane * 16, (eb * 8 + u) * 1024, AUX_HO));
        bar();
        const u32 bb = base + q * YQ + c * 96;
        f32x4 acc[2][6] = {};
#pragma unroll
        for (int st = 0; st < 10; ++st) {
            bar();
#pragma unroll
            for (int ks = 0; ks < 2; ++ks) {
                const int eb = 2 * st + ks;
                bf16x8 b[6];
#pragma unroll
                for (int n = 0; n < 6; ++n) b[n] = ldsr128(bb + eb * YEB + n * 16);
#pragma unroll
                for (int n = 0; n < 6; ++n)
#pragma unroll
                    for (int u = 0; u < 2; ++u) acc[u][n] = MFMA(xa[eb][u], b[n], acc[u][n]);
            }
        }
        const rsrc_t ry = mk(p.y, S_ * F_ * 2);
        const u32 yo = ((sb * 128 + 32 * wave + 4 * q) * F_ + gb * 96 + 6 * c) * 2;
#pragma unroll
        for (int u = 0; u < 2; ++u)
#pragma unroll
            for (int r = 0; r < 4; ++r) {
                u32x3 v;
                v[0] = pk(acc[u][0][r], acc[u][1][r]);
                v[1] = pk(acc[u][2][r], acc[u][3][r]);
                v[2] = pk(acc[u][4][r], acc[u][5][r]);
                __builtin_amdgcn_raw_buffer_store_b96(v, ry, yo, (16 * u + r) * F_ * 2, 0);
            }
    }
}

template <int MODE>
__global__ __launch_bounds__(512, 2) void quip_kernel(Params p) {
    const int b = blockIdx.x, xcd = b & 7, i = b >> 3;
    const int lane = threadIdx.x & 63;
    const int wave = __builtin_amdgcn_readfirstlane(threadIdx.x >> 6);
    u64 tag = 0;
    if (MODE != 2) {
        int fidx;
        if (i < 20) {
            const int half = xcd & 1, et = 5 * ((xcd >> 1) & 1) + i % 5, gt = 4 * (xcd >> 2) + i / 5;
            m_role(p, wave, lane, et, gt, half);
            fidx = gt * 32 + et * 2 + half;
        } else if (i < 30) {
            const int j = i - 20, sb = 2 * xcd + j / 5, eblk = j % 5;
            x_role(p, wave, lane, sb, eblk);
            fidx = 256 + sb * 8 + eblk;
        } else {
            fidx = 384 + (b - 240);
        }
        if (MODE == 0) {
            if (wave >= 4) tag = __hip_atomic_load((const u64*)((const char*)p.ws + EP_OFF), __ATOMIC_RELAXED, __HIP_MEMORY_SCOPE_AGENT) + TAGC;
            waitv<0>();
            bar();
            if (wave == 4 && lane == 0) __hip_atomic_store((u64*)((char*)p.ws + FL_OFF) + fidx, tag, __ATOMIC_RELAXED, __HIP_MEMORY_SCOPE_AGENT);
        }
    }
    if (MODE != 1) {
        const int sb = 8 * (xcd & 1) + (i & 7), gb = 4 * (xcd >> 1) + (i >> 3);
        y_role(p, wave, lane, sb, gb, tag, b == 255);
    }
}

extern "C" void kernel_launch(void* const* d_in, const int* in_sizes, int n_in, void* d_out, int out_size, void* d_ws,
                              size_t ws_size, hipStream_t stream) {
    Params p;
    p.x = d_in[0]; p.qw = d_in[1]; p.sc = d_in[2]; p.ze = d_in[3]; p.U = d_in[4]; p.V = d_in[5];
    p.y = d_out; p.ws = d_ws;
    quip_kernel<0><<<256, 512, 0, stream>>>(p);
}
```

```cpp
#include <hip/hip_runtime.h>
#include <stdint.h>
#pragma clang diagnostic ignored "-Wint-to-pointer-cast"

#define MLA 20
#define XA 2
#define YLA 8
#define YR 6
#define XTH 8

typedef unsigned u32;
typedef unsigned long long u64;
using bf16x8 = __attribute__((ext_vector_type(8))) short;
using s16x4 = __attribute__((ext_vector_type(4))) short;
using f32x4 = __attribute__((ext_vector_type(4))) float;
using u32x4 = __attribute__((ext_vector_type(4))) unsigned;
using u32x3 = __attribute__((ext_vector_type(3))) unsigned;
using u32x2 = __attribute__((ext_vector_type(2))) unsigned;
using bf16x2_t = __attribute__((ext_vector_type(2))) __bf16;
using f32x2 = __attribute__((ext_vector_type(2))) float;
#define LAS __attribute__((address_space(3)))
typedef __amdgpu_buffer_rsrc_t rsrc_t;

constexpr int S_ = 2048, D_ = 640, F_ = 1536;
constexpr u32 XF_OFF = 0, XF_BYTES = S_ * D_ * 2;
constexpr u32 MF_OFF = XF_BYTES, MF_BYTES = 2 * D_ * F_ * 2;
constexpr u32 FL_OFF = MF_OFF + MF_BYTES;
constexpr u32 EP_OFF = FL_OFF + 4096;
constexpr u32 WS_BYTES = EP_OFF + 256;
constexpr int LDS_BYTES = 163840;
constexpr u32 CTL_OFF = 162816;
constexpr u32 TOP_OFF = 129024;
constexpr u64 TAGC = 0x9E3779B97F4A7C15ull;

struct Params {
    const void *x, *qw, *sc, *ze, *U, *V;
    void *y, *ws;
};

__device__ __forceinline__ rsrc_t mk(const void* p, u32 bytes) { return __builtin_amdgcn_make_buffer_rsrc((void*)p, (short)0, (int)bytes, 0x00020000); }
__device__ __forceinline__ void glds(u32 m0v, u32 voff, rsrc_t r, u32 soff) {
    asm volatile("s_mov_b32 m0, %0\n\ts_nop 0\n\tbuffer_load_dwordx4 %1, %2, %3 offen lds" ::"s"(m0v), "v"(voff), "s"(r), "s"(soff) : "memory");
}
__device__ __forceinline__ void gld(u32x4& d, u32 voff, rsrc_t r, u32 soff) {
    asm volatile("buffer_load_dwordx4 %0, %1, %2, %3 offen" : "=v"(d) : "v"(voff), "s"(r), "s"(soff) : "memory");
}
__device__ __forceinline__ void gld_ho(u32x4& d, u32 voff, rsrc_t r, u32 soff) {
    asm volatile("buffer_load_dwordx4 %0, %1, %2, %3 offen sc1" : "=v"(d) : "v"(voff), "s"(r), "s"(soff) : "memory");
}
__device__ __forceinline__ void gld_ho2(u32x2& d, u32 voff, rsrc_t r, u32 soff) {
    asm volatile("buffer_load_dwordx2 %0, %1, %2, %3 offen sc1" : "=v"(d) : "v"(voff), "s"(r), "s"(soff) : "memory");
}
template <int N> __device__ __forceinline__ void waitv() { asm volatile("s_waitcnt vmcnt(%0)" ::"n"(N) : "memory"); }
template <int N, class T> __device__ __forceinline__ void waitv1(T& a) { asm volatile("s_waitcnt vmcnt(%1)" : "+v"(a) : "n"(N) : "memory"); }
template <int N, class T> __device__ __forceinline__ void waitv2(T& a, T& b) { asm volatile("s_waitcnt vmcnt(%2)" : "+v"(a), "+v"(b) : "n"(N) : "memory"); }
__device__ __forceinline__ void barw() { asm volatile("s_waitcnt lgkmcnt(0)\n\ts_barrier" ::: "memory"); }
__device__ __forceinline__ void barn() { asm volatile("s_barrier" ::: "memory"); }
#define SB() __builtin_amdgcn_sched_barrier(0)
__device__ __forceinline__ u32 pk(float a, float b) {
    f32x2 v = {a, b};
    return __builtin_bit_cast(u32, __builtin_convertvector(v, bf16x2_t));
}
__device__ __forceinline__ float fadd(float a, float b) { float d; asm("v_add_f32 %0, %1, %2" : "=v"(d) : "v"(a), "v"(b)); return d; }
__device__ __forceinline__ float ffma(float a, float b, float c) { float d; asm("v_fma_f32 %0, %1, %2, %3" : "=v"(d) : "v"(a), "v"(b), "v"(c)); return d; }
__device__ __forceinline__ bf16x8 ldsr128(u32 a) { return *(const LAS bf16x8*)a; }
__device__ __forceinline__ s16x4 ldstr(u32 a) { return __builtin_amdgcn_ds_read_tr16_b64_v4i16((LAS s16x4*)a); }
__device__ __forceinline__ bf16x8 cat(s16x4 a, s16x4 b) { return __builtin_shufflevector(a, b, 0, 1, 2, 3, 4, 5, 6, 7); }
#define MFMA(a, b, c) __builtin_amdgcn_mfma_f32_16x16x32_bf16(a, b, c, 0, 0, 0)

__shared__ __attribute__((aligned(1024))) char smem[LDS_BYTES];

struct Stamps {};
#define STAMP(k) do {} while (0)

__device__ __forceinline__ u64 ld_epoch(const Params& p) {
    return __hip_atomic_load((const u64*)((const char*)p.ws + EP_OFF), __ATOMIC_RELAXED, __HIP_MEMORY_SCOPE_AGENT);
}
__device__ __forceinline__ void put_ctl(u64 e) {
    const u32 base = (u32)(uintptr_t)smem;
    *(LAS u64*)(base + CTL_OFF) = e + TAGC;
    *(LAS u32*)(base + CTL_OFF + 8) = 0;
    *(LAS u32x2*)(base + CTL_OFF + 16) = u32x2{0, 0};
}
__device__ __forceinline__ u64 get_tag() { return *(const LAS u64*)((u32)(uintptr_t)smem + CTL_OFF); }
__device__ __forceinline__ void publish(const Params& p, int lane, int fidx, u64 tag) {
    waitv<0>();
    if (lane == 0) {
        const u32 old = __hip_atomic_fetch_add((LAS u32*)((u32)(uintptr_t)smem + CTL_OFF + 8), 1u, __ATOMIC_RELAXED, __HIP_MEMORY_SCOPE_WORKGROUP);
        if (old == 3) __hip_atomic_store((u64*)((char*)p.ws + FL_OFF) + fidx, tag, __ATOMIC_RELAXED, __HIP_MEMORY_SCOPE_AGENT);
    }
}
__device__ __forceinline__ void poll(const u64* f, bool active, u64 tag) {
    for (u32 spins = 0; spins < (1u << 16); ++spins) {
        u64 v = tag;
        if (active) v = __hip_atomic_load(f, __ATOMIC_RELAXED, __HIP_MEMORY_SCOPE_AGENT);
        if (__all(v == tag)) return;
        __builtin_amdgcn_s_sleep(1);
    }
}

__device__ __forceinline__ void wait_ready(bool leader, const u64* f, bool active, u64 tag, u32 word) {
    LAS u32* w = (LAS u32*)((u32)(uintptr_t)smem + CTL_OFF + word);
    if (leader) {
        poll(f, active, tag);
        __hip_atomic_store(w, 1u, __ATOMIC_RELAXED, __HIP_MEMORY_SCOPE_WORKGROUP);
    } else {
        for (u32 spins = 0; spins < (1u << 22); ++spins) {
            if (__builtin_amdgcn_readfirstlane(__hip_atomic_load(w, __ATOMIC_RELAXED, __HIP_MEMORY_SCOPE_WORKGROUP))) break;
            __builtin_amdgcn_s_sleep(1);
        }
    }
    asm volatile("" ::: "memory");
}

constexpr u32 mbslot(int st) { return st % 4 == 3 ? TOP_OFF : (st % 4) * 24576; }
constexpr u32 maslot(int st) { return st % 3 == 2 ? TOP_OFF + 24576 : 73728 + (st % 3) * 8192; }
constexpr u32 MS_OFF = 90112, MZ_OFF = MS_OFF + 3072;
static_assert(MLA <= 20, "slot reuse");
#define MROT 0
constexpr int MAR = 8;
struct MCtx { u32 voffA, voffB[6], wa[4], wb, sca; rsrc_t rq, rv; int rot; };
__device__ __forceinline__ u32 krot(int st, int rot) {
    if (MROT == 0) return st;
    const int k = st + rot;
    return k >= 12 ? k - 12 : k;
}
#define MORD 0
constexpr int mord(int k) {
    constexpr int o0[10] = {0, ~0, 1, ~1, 2, 3, ~2, 4, ~3, 5};
    constexpr int o1[10] = {~0, 0, ~1, 1, ~2, 2, ~3, 3, 4, 5};
    constexpr int o2[10] = {~0, ~1, ~2, ~3, 0, 1, 2, 3, 4, 5};
    return MORD == 0 ? o0[k] : MORD == 1 ? o1[k] : o2[k];
}
template <int P> struct MP {
    static constexpr int ST = P / 10, K = P % 10;
    static constexpr bool isA = mord(K) < 0;
    static constexpr int idx = isA ? ~mord(K) : mord(K);
    static constexpr int ar = (ST * 4 + idx) % MAR;
    static __device__ __forceinline__ void issue(u32x4 (&r)[MAR], const MCtx& c) {
        if constexpr (isA) gld(r[ar], c.voffA, c.rq, krot(ST, c.rot) * 256 + idx * 24576);
        else glds(c.wb + mbslot(ST) + idx * 1024, c.voffB[idx], c.rv, krot(ST, c.rot) * 196608);
    }
    static __device__ __forceinline__ void pro(u32x4 (&r)[MAR], const MCtx& c) {
        issue(r, c);
        if constexpr (P + 1 < MLA) MP<P + 1>::pro(r, c);
    }
    static __device__ __forceinline__ void run(u32x4 (&r)[MAR], const MCtx& c, f32x4& s4, f32x4& z4, Stamps& stp) {
        constexpr int REM = (119 - P) < (MLA - 1) ? (119 - P) : (MLA - 1);
        if constexpr (K == 0) {
            s4 = *(const LAS f32x4*)(c.sca + MS_OFF + krot(ST, c.rot) * 256);
            z4 = *(const LAS f32x4*)(c.sca + MZ_OFF + krot(ST, c.rot) * 256);
        }
        if constexpr (isA) {
            waitv1<REM>(r[ar]);
            const u32x4 q = r[ar];
            u32x2 o;
            o[0] = pk(ffma((float)(int)q[0], s4[0], z4[0]), ffma((float)(int)q[1], s4[1], z4[1]));
            o[1] = pk(ffma((float)(int)q[2], s4[2], z4[2]), ffma((float)(int)q[3], s4[3], z4[3]));
            *(LAS u32x2*)(c.wa[idx] + maslot(ST)) = o;
        } else if constexpr (K == 9) {
            waitv<REM>();
        }
        if constexpr (K == 9) { barw(); STAMP(2 + ST); }
        if constexpr (P + MLA < 120) MP<P + MLA>::issue(r, c);
        if constexpr (P + 1 < 120) MP<P + 1>::run(r, c, s4, z4, stp);
    }
};

struct MSet { bf16x8 a[4]; s16x4 bl[3], bh[3]; };
template <bool DOM, bool DOR>
__device__ __forceinline__ void m_phase(f32x4 (&acc)[4][3], const MSet& m, MSet& r, u32 aa, const u32 (&bb)[3], u32 aoff, u32 boff) {
#pragma unroll
    for (int i = 0; i < 12; ++i) {
        if (DOM) acc[i / 3][i % 3] = MFMA(m.a[i / 3], cat(m.bl[i % 3], m.bh[i % 3]), acc[i / 3][i % 3]);
        if (DOR) {
            if (i == 0) r.bl[0] = ldstr(bb[0] + boff);
            if (i == 1) r.bh[0] = ldstr(bb[0] + boff + 1536);
            if (i == 2) r.a[0] = ldsr128(aa + aoff);
            if (i == 3) r.bl[1] = ldstr(bb[1] + boff);
            if (i == 4) r.bh[1] = ldstr(bb[1] + boff + 1536);
            if (i == 5) r.bl[2] = ldstr(bb[2] + boff);
            if (i == 6) r.bh[2] = ldstr(bb[2] + boff + 1536);
            if (i >= 7 && i < 10) r.a[i - 6] = ldsr128(aa + aoff + (i - 6) * 2048);
        }
        SB();
    }
}

__device__ __forceinline__ void m_prod(const Params& p, int pw, int lane, int et, int gt, int half, Stamps& stp) {
    const u32 base = (u32)(uintptr_t)smem;
    const int q = lane >> 4, c = lane & 15;
    const int kb = half * 768;
    MCtx cx;
    cx.rq = mk(p.qw, D_ * F_ * 4); cx.rv = mk(p.V, F_ * F_ * 2);
    cx.voffA = ((et * 64 + 16 * pw + q) * F_ + kb + 4 * c) * 4;
#pragma unroll
    for (int j = 0; j < 6; ++j) {
        const int P = (6 * pw + j) * 1024 + 16 * lane;
        const int r = P / 384, o = P - r * 384;
        const int sw = ((r >> 1) & 1) | (((r >> 3) & 1) << 1);
        cx.voffB[j] = (kb + r) * (F_ * 2) + gt * 384 + (((o >> 5) ^ sw) << 5) + (o & 16);
    }
#pragma unroll
    for (int i = 0; i < 4; ++i) {
        const int row = 16 * pw + 4 * i + q;
        cx.wa[i] = base + row * 128 + (((c >> 1) ^ ((row >> 1) & 7)) << 4) + (c & 1) * 8;
    }
    cx.wb = base + pw * 6144;
    cx.sca = base + c * 16;
    cx.rot = MROT == 1 ? et % 5 : MROT == 2 ? 2 * (et % 5) : MROT == 3 ? ((et % 5) + 3 * (gt & 3)) % 12 : MROT == 4 ? (gt & 3) : 0;
    u32x4 r[MAR];
    MP<0>::pro(r, cx);
    STAMP(1);
    barn();
    f32x4 s4, z4;
    MP<0>::run(r, cx, s4, z4, stp);
}

__device__ __forceinline__ void m_cons(const Params& p, int wave, int lane, int et, int gt, int half, Stamps& stp) {
    const u32 base = (u32)(uintptr_t)smem;
    const int q = lane >> 4, c = lane & 15;
    const int kb = half * 768;
    {
        const int t = wave * 64 + lane;
        if (t < 192) {
            const u32x2 sv = *(const u32x2*)((const char*)p.sc + (kb + 4 * t) * 2);
            const u32x2 zv = *(const u32x2*)((const char*)p.ze + (kb + 4 * t) * 2);
            f32x4 s4, z4;
            s4[0] = __uint_as_float(sv[0] << 16); s4[1] = __uint_as_float(sv[0] & 0xffff0000u);
            s4[2] = __uint_as_float(sv[1] << 16); s4[3] = __uint_as_float(sv[1] & 0xffff0000u);
            z4[0] = -__uint_as_float(zv[0] << 16) * s4[0]; z4[1] = -__uint_as_float(zv[0] & 0xffff0000u) * s4[1];
            z4[2] = -__uint_as_float(zv[1] << 16) * s4[2]; z4[3] = -__uint_as_float(zv[1] & 0xffff0000u) * s4[3];
            *(LAS f32x4*)(base + MS_OFF + t * 16) = s4;
            *(LAS f32x4*)(base + MZ_OFF + t * 16) = z4;
        } else if (t == 192) {
            put_ctl(ld_epoch(p));
        }
    }
    barw();
    STAMP(1);
    const int qq = c >> 2, pp = c & 3;
    const int sw = (qq >> 1) | ((q & 1) << 1);
    u32 ab[2], bb[3];
    ab[0] = base + c * 128 + ((q ^ ((c >> 1) & 7)) << 4);
    ab[1] = ab[0] ^ 64;
#pragma unroll
    for (int n = 0; n < 3; ++n) bb[n] = base + (8 * q + qq) * 384 + (((3 * wave + n) ^ sw) << 5) + 8 * pp;
    f32x4 acc[4][3] = {};
    MSet s0, s1;
#pragma unroll
    for (int st = 0; st < 12; ++st) {
        SB(); barn(); SB();
        if (st == 0) m_phase<false, true>(acc, s1, s0, ab[0], bb, maslot(st), mbslot(st));
        else m_phase<true, true>(acc, s1, s0, ab[0], bb, maslot(st), mbslot(st));
        m_phase<true, true>(acc, s0, s1, ab[1], bb, maslot(st), mbslot(st) + 12288);
    }
    m_phase<true, false>(acc, s1, s0, 0, bb, 0, 0);
    const rsrc_t rw = mk(p.ws, WS_BYTES);
#pragma unroll
    for (int ebl = 0; ebl < 2; ++ebl)
#pragma unroll
        for (int n = 0; n < 3; ++n) {
            const int nt = 3 * wave + n;
            const int gb = 2 * gt + nt / 6, np = nt % 6;
            u32x4 v;
            v[0] = pk(acc[2 * ebl][n][0], acc[2 * ebl][n][1]);
            v[1] = pk(acc[2 * ebl][n][2], acc[2 * ebl][n][3]);
            v[2] = pk(acc[2 * ebl + 1][n][0], acc[2 * ebl + 1][n][1]);
            v[3] = pk(acc[2 * ebl + 1][n][2], acc[2 * ebl + 1][n][3]);
            const u32 off = MF_OFF + (((half * 16 + gb) * 20 + 2 * et + ebl) * 6 + np) * 1024;
            __builtin_amdgcn_raw_buffer_store_b128(v, rw, lane * 16, off, 16);
        }
}

constexpr u32 xslot(int st) { return st % 4 == 1 ? TOP_OFF : st % 4 == 0 ? 0 : (st % 4 - 1) * 32768; }
struct XCtx { u32 vU[2], vX[2], ldsb; rsrc_t ru, rx; };
template <int ST> struct XP {
    static __device__ __forceinline__ void issue(const XCtx& c) {
#pragma unroll
        for (int j = 0; j < 4; ++j) {
            glds(c.ldsb + xslot(ST) + 16384 + j * 1024, c.vX[j & 1], c.rx, ST * 128 + j * 8 * 1280);
            glds(c.ldsb + xslot(ST) + j * 1024, c.vU[j >> 1], c.ru, ST * 64 * 1280 + j * 4 * 1280);
        }
    }
    static __device__ __forceinline__ void pro(const XCtx& c) {
        issue(c);
        if constexpr (ST + 1 < XA) XP<ST + 1>::pro(c);
    }
    static __device__ __forceinline__ void run(const XCtx& c, Stamps& stp) {
        constexpr int REM = (9 - ST) < (XA - 1) ? (9 - ST) : (XA - 1);
        waitv<REM * 8>();
        barn();
        STAMP(2 + ST);
        if constexpr (ST + XA < 10) XP<ST + XA>::issue(c);
        if constexpr (ST + 1 < 10) XP<ST + 1>::run(c, stp);
    }
};
static_assert(XA <= 2, "slot reuse");

struct XSet { s16x4 al[4], ah[4]; bf16x8 b[4]; };
template <bool DOM, bool DOR>
__device__ __forceinline__ void x_phase(f32x4 (&acc)[4][4], const XSet& m, XSet& r, const u32 (&ab)[4], u32 bb, u32 aoff, u32 boff) {
#pragma unroll
    for (int i = 0; i < 16; ++i) {
        if (DOM) acc[i / 4][i % 4] = MFMA(cat(m.al[i / 4], m.ah[i / 4]), m.b[i % 4], acc[i / 4][i % 4]);
        if (DOR) {
            if (i == 0) r.al[0] = ldstr(ab[0] + aoff);
            if (i == 1) r.ah[0] = ldstr(ab[0] + aoff + 1024);
            if (i >= 2 && i < 6) r.b[i - 2] = ldsr128(bb + boff + (i - 2) * 2048);
            if (i >= 6 && i < 12 && !(i & 1)) r.al[(i - 4) / 2] = ldstr(ab[(i - 4) / 2] + aoff);
            if (i >= 6 && i < 12 && (i & 1)) r.ah[(i - 4) / 2] = ldstr(ab[(i - 4) / 2] + aoff + 1024);
        }
        SB();
    }
}

__device__ __forceinline__ void x_prod(const Params& p, int pw, int lane, int sb, int eblk, Stamps& stp) {
    const u32 base = (u32)(uintptr_t)smem;
    const int q = lane >> 4, c = lane & 15;
    XCtx cx;
    cx.ru = mk(p.U, D_ * D_ * 2); cx.rx = mk(p.x, S_ * D_ * 2);
#pragma unroll
    for (int h = 0; h < 2; ++h) {
        cx.vU[h] = (16 * pw + q) * 1280 + eblk * 256 + (((c >> 1) ^ (q | (h << 2))) << 5) + (c & 1) * 16;
        cx.vX[h] = (sb * 128 + 32 * pw + (lane >> 3)) * 1280 + (((lane & 7) ^ ((4 * h + q) & 7)) << 4);
    }
    cx.ldsb = base + pw * 4096;
    XP<0>::pro(cx);
    STAMP(1);
    XP<0>::run(cx, stp);
}

__device__ __forceinline__ void x_cons(const Params& p, int wave, int lane, int sb, int eblk, Stamps& stp) {
    const u32 base = (u32)(uintptr_t)smem;
    const int q = lane >> 4, c = lane & 15;
    const int we = wave & 1, ws = wave >> 1;
    const int qq = c >> 2, pp = c & 3;
    const int sw = qq | ((q & 1) << 2);
    u64 ep = 0;
    if (wave == 0 && lane == 0) ep = ld_epoch(p);
    u32 ab[4], bb[2];
#pragma unroll
    for (int t = 0; t < 4; ++t) ab[t] = base + (8 * q + qq) * 256 + (((4 * we + t) ^ sw) << 5) + 8 * pp;
    bb[0] = base + 16384 + (64 * ws + c) * 128 + ((q ^ ((c >> 1) & 7)) << 4);
    bb[1] = bb[0] ^ 64;
    f32x4 acc[4][4] = {};
    XSet s0, s1;
#pragma unroll
    for (int st = 0; st < 10; ++st) {
        if (st == 2 && wave == 0 && lane == 0) put_ctl(ep);
        SB(); barn(); SB();
        if (st == 0) x_phase<false, true>(acc, s1, s0, ab, bb[0], xslot(st), xslot(st));
        else x_phase<true, true>(acc, s1, s0, ab, bb[0], xslot(st), xslot(st));
        x_phase<true, true>(acc, s0, s1, ab, bb[1], xslot(st) + 8192, xslot(st));
    }
    x_phase<true, false>(acc, s1, s0, ab, 0, 0, 0);
    const rsrc_t rw = mk(p.ws, WS_BYTES);
#pragma unroll
    for (int ebl = 0; ebl < 2; ++ebl)
#pragma unroll
        for (int u = 0; u < 4; ++u) {
            u32x4 v;
            v[0] = pk(acc[2 * ebl][u][0], acc[2 * ebl][u][1]);
            v[1] = pk(acc[2 * ebl][u][2], acc[2 * ebl][u][3]);
            v[2] = pk(acc[2 * ebl + 1][u][0], acc[2 * ebl + 1][u][1]);
            v[3] = pk(acc[2 * ebl + 1][u][2], acc[2 * ebl + 1][u][3]);
            const u32 off = XF_OFF + ((sb * 20 + 4 * eblk + 2 * we + ebl) * 8 + 4 * ws + u) * 1024;
            __builtin_amdgcn_raw_buffer_store_b128(v, rw, lane * 16, off, 16);
        }
}

constexpr u32 YQ = 1552, YEB = 4 * YQ;
__device__ __forceinline__ u32 add2(u32 a, u32 b) {
    const float lo = fadd(__uint_as_float(a << 16), __uint_as_float(b << 16));
    const float hi = fadd(__uint_as_float(a & 0xffff0000u), __uint_as_float(b & 0xffff0000u));
    return pk(lo, hi);
}
struct YCtx { u32 voff, wa[3]; rsrc_t rw; };
template <int P> struct YP {
    static constexpr int ST = P / 3, J = P % 3;
    static __device__ __forceinline__ void issue(u32x4 (&r)[YLA][2], const YCtx& c) {
        gld_ho(r[P % YLA][0], c.voff, c.rw, (ST * 12 + J) * 1024);
        gld_ho(r[P % YLA][1], c.voff, c.rw, (ST * 12 + J) * 1024 + 16 * 120 * 1024);
    }
    static __device__ __forceinline__ void pro(u32x4 (&r)[YLA][2], const YCtx& c) {
        issue(r, c);
        if constexpr (P + 1 < YLA) YP<P + 1>::pro(r, c);
    }
    static __device__ __forceinline__ void run(u32x4 (&r)[YLA][2], const YCtx& c, Stamps& stp) {
        constexpr int REM = (29 - P) < (YLA - 1) ? (29 - P) : (YLA - 1);
        waitv2<REM * 2>(r[P % YLA][0], r[P % YLA][1]);
        u32x4 o;
#pragma unroll
        for (int e = 0; e < 4; ++e) o[e] = add2(r[P % YLA][0][e], r[P % YLA][1][e]);
        *(LAS u32x4*)(c.wa[J] + ST * 2 * YEB) = o;
        if constexpr (P + YLA < 30) YP<P + YLA>::issue(r, c);
        if constexpr (J == 2) { barw(); STAMP(20 + ST); }
        if constexpr (P + 1 < 30) YP<P + 1>::run(r, c, stp);
    }
};

__device__ __forceinline__ void y_load(const Params& p, int pw, int lane, int gb, u64 tag, bool updater, Stamps& stp) {
    const u32 base = (u32)(uintptr_t)smem;
    const int q = lane >> 4, c = lane & 15;
    YCtx cx;
    cx.rw = mk(p.ws, WS_BYTES);
    cx.voff = MF_OFF + (gb * 120 + 3 * pw) * 1024 + lane * 16;
#pragma unroll
    for (int j = 0; j < 3; ++j) {
        const int ci = 3 * pw + j;
        cx.wa[j] = base + (ci / 6) * YEB + q * YQ + (16 * (ci % 6) + c) * 16;
    }
    const u64* fl = (const u64*)((const char*)p.ws + FL_OFF);
    wait_ready(pw == 0, fl + (gb >> 1) * 20 + lane, lane < 20, tag, 16);
    STAMP(19);
    u32x4 r[YLA][2];
    YP<0>::pro(r, cx);
    YP<0>::run(r, cx, stp);
    if (updater && pw == 0) {
#pragma unroll 1
        for (u32 spins = 0; spins < (1u << 16); ++spins) {
            u32x4 f0, f1;
            gld_ho(f0, FL_OFF + lane * 32, cx.rw, 0);
            gld_ho(f1, FL_OFF + lane * 32 + 16, cx.rw, 0);
            waitv2<0>(f0, f1);
            const u32 tl = (u32)tag, th = (u32)(tag >> 32);
            const bool ok = f0[0] == tl && f0[1] == th && f0[2] == tl && f0[3] == th && f1[0] == tl && f1[1] == th && f1[2] == tl && f1[3] == th;
            if (__all(ok)) break;
            __builtin_amdgcn_s_sleep(1);
        }
        if (lane == 0) __hip_atomic_store((u64*)((char*)p.ws + EP_OFF), tag, __ATOMIC_RELAXED, __HIP_MEMORY_SCOPE_AGENT);
    }
}

__device__ __forceinline__ void y_mma(const Params& p, int w, int lane, int sb, int gb, u64 tag, Stamps& stp) {
    const u32 base = (u32)(uintptr_t)smem;
    const int q = lane >> 4, c = lane & 15;
    const rsrc_t rw = mk(p.ws, WS_BYTES);
    const u64* fl = (const u64*)((const char*)p.ws + FL_OFF);
    wait_ready(w == 0, fl + 160 + sb * 5 + lane, lane < 5, tag, 20);
    STAMP(18);
    bf16x8 xa[20][2];
#pragma unroll
    for (int eb = 0; eb < 20; ++eb) {
#pragma unroll
        for (int u = 0; u < 2; ++u)
            xa[eb][u] = __builtin_bit_cast(bf16x8, __builtin_amdgcn_raw_buffer_load_b128(rw, XF_OFF + (sb * 160 + 2 * w) * 1024 + lane * 16, (eb * 8 + u) * 1024, 16));
        if (2 * eb + 2 > XTH) waitv<XTH>();
    }
    STAMP(19);
    const u32 bb = base + q * YQ + c * 96;
    f32x4 acc[2][6] = {};
    bf16x8 b[YR];
    SB(); barn(); SB();
#pragma unroll
    for (int f = 0; f < YR; ++f) b[f] = ldsr128(bb + (f / 6) * YEB + (f % 6) * 16);
    SB();
#pragma unroll
    for (int f = 0; f < 120; ++f) {
        const int eb = f / 6, n = f % 6;
        acc[0][n] = MFMA(xa[eb][0], b[f % YR], acc[0][n]);
        acc[1][n] = MFMA(xa[eb][1], b[f % YR], acc[1][n]);
        SB();
        if (f + YR < 120) {
            if ((f + YR) % 12 == 0) { barn(); SB(); }
            b[f % YR] = ldsr128(bb + ((f + YR) / 6) * YEB + ((f + YR) % 6) * 16);
            SB();
        }
    }
    STAMP(30);
    const rsrc_t ry = mk(p.y, S_ * F_ * 2);
    const u32 yo = ((sb * 128 + 32 * w + 4 * q) * F_ + gb * 96 + 6 * c) * 2;
#pragma unroll
    for (int u = 0; u < 2; ++u)
#pragma unroll
        for (int r = 0; r < 4; ++r) {
            u32x3 v;
            v[0] = pk(acc[u][0][r], acc[u][1][r]);
            v[1] = pk(acc[u][2][r], acc[u][3][r]);
            v[2] = pk(acc[u][4][r], acc[u][5][r]);
            __builtin_amdgcn_raw_buffer_store_b96(v, ry, yo, (16 * u + r) * F_ * 2, 2);
        }
}

__global__ __launch_bounds__(512, 2) void quip_kernel(Params p) {
    const int b = blockIdx.x, xcd = b & 7, i = b >> 3;
    const int lane = threadIdx.x & 63;
    const int wave = __builtin_amdgcn_readfirstlane(threadIdx.x >> 6);
    Stamps stp;
    const int sb2 = 8 * (xcd & 1) + (i & 7), gb2 = 4 * (xcd >> 1) + (i >> 3);
    const int half = xcd & 1, et = 5 * ((xcd >> 1) & 1) + i % 5, gt = 4 * (xcd >> 2) + i / 5;
    const int j = i - 20, sb = 2 * xcd + j / 5, eblk = j % 5;
    if (wave >= 4) {
        if (i < 20) m_prod(p, wave - 4, lane, et, gt, half, stp);
        else if (i < 30) x_prod(p, wave - 4, lane, sb, eblk, stp);
        else barn();
        STAMP(15);
        y_mma(p, wave - 4, lane, sb2, gb2, get_tag(), stp);
    } else {
        int fidx;
        if (i < 20) { m_cons(p, wave, lane, et, gt, half, stp); fidx = gt * 20 + et * 2 + half; }
        else if (i < 30) { x_cons(p, wave, lane, sb, eblk, stp); fidx = 160 + sb * 5 + eblk; }
        else {
            if (wave == 0 && lane == 0) put_ctl(ld_epoch(p));
            barw();
            fidx = b;
        }
        STAMP(15);
        const u64 tag = get_tag();
        publish(p, lane, fidx, tag);
        STAMP(17);
        y_load(p, wave, lane, gb2, tag, b == 255, stp);
    }
}

extern "C" void kernel_launch(void* const* d_in, const int* in_sizes, int n_in, void* d_out, int out_size, void* d_ws,
                              size_t ws_size, hipStream_t stream) {
    Params p;
    p.x = d_in[0]; p.qw = d_in[1]; p.sc = d_in[2]; p.ze = d_in[3]; p.U = d_in[4]; p.V = d_in[5];
    p.y = d_out; p.ws = d_ws;
    quip_kernel<<<256, 512, 0, stream>>>(p);
}
```

```cpp
#include <hip/hip_runtime.h>
#include <stdint.h>
#pragma clang diagnostic ignored "-Wint-to-pointer-cast"

#define MLA 20
#define XA 2
#define YLA 8
#define YR 6
#define XTH 8

#define CPFW 3
#define CPFN 7
typedef unsigned u32;
typedef unsigned long long u64;
using bf16x8 = __attribute__((ext_vector_type(8))) short;
using s16x4 = __attribute__((ext_vector_type(4))) short;
using f32x4 = __attribute__((ext_vector_type(4))) float;
using u32x4 = __attribute__((ext_vector_type(4))) unsigned;
using u32x3 = __attribute__((ext_vector_type(3))) unsigned;
using u32x2 = __attribute__((ext_vector_type(2))) unsigned;
using bf16x2_t = __attribute__((ext_vector_type(2))) __bf16;
using f32x2 = __attribute__((ext_vector_type(2))) float;
#define LAS __attribute__((address_space(3)))
typedef __amdgpu_buffer_rsrc_t rsrc_t;

constexpr int S_ = 2048, D_ = 640, F_ = 1536;
constexpr u32 XF_OFF = 0, XF_BYTES = S_ * D_ * 2;
constexpr u32 MF_OFF = XF_BYTES, MF_BYTES = 2 * D_ * F_ * 2;
constexpr u32 FL_OFF = MF_OFF + MF_BYTES;
constexpr u32 EP_OFF = FL_OFF + 4096;
constexpr u32 WS_BYTES = EP_OFF + 256;
constexpr int LDS_BYTES = 163840;
constexpr u32 CTL_OFF = 162816;
constexpr u32 TOP_OFF = 129024;
constexpr u64 TAGC = 0x9E3779B97F4A7C15ull;

struct Params {
    const void *x, *qw, *sc, *ze, *U, *V;
    void *y, *ws;
};

__device__ __forceinline__ rsrc_t mk(const void* p, u32 bytes) { return __builtin_amdgcn_make_buffer_rsrc((void*)p, (short)0, (int)bytes, 0x00020000); }
__device__ __forceinline__ void glds(u32 m0v, u32 voff, rsrc_t r, u32 soff) {
    asm volatile("s_mov_b32 m0, %0\n\ts_nop 0\n\tbuffer_load_dwordx4 %1, %2, %3 offen lds" ::"s"(m0v), "v"(voff), "s"(r), "s"(soff) : "memory");
}
__device__ __forceinline__ void gld(u32x4& d, u32 voff, rsrc_t r, u32 soff) {
    asm volatile("buffer_load_dwordx4 %0, %1, %2, %3 offen" : "=v"(d) : "v"(voff), "s"(r), "s"(soff) : "memory");
}
__device__ __forceinline__ void gld_ho(u32x4& d, u32 voff, rsrc_t r, u32 soff) {
    asm volatile("buffer_load_dwordx4 %0, %1, %2, %3 offen sc1" : "=v"(d) : "v"(voff), "s"(r), "s"(soff) : "memory");
}
__device__ __forceinline__ void gld_ho2(u32x2& d, u32 voff, rsrc_t r, u32 soff) {
    asm volatile("buffer_load_dwordx2 %0, %1, %2, %3 offen sc1" : "=v"(d) : "v"(voff), "s"(r), "s"(soff) : "memory");
}
template <int N> __device__ __forceinline__ void waitv() { asm volatile("s_waitcnt vmcnt(%0)" ::"n"(N) : "memory"); }
template <int N, class T> __device__ __forceinline__ void waitv1(T& a) { asm volatile("s_waitcnt vmcnt(%1)" : "+v"(a) : "n"(N) : "memory"); }
template <int N, class T> __device__ __forceinline__ void waitv2(T& a, T& b) { asm volatile("s_waitcnt vmcnt(%2)" : "+v"(a), "+v"(b) : "n"(N) : "memory"); }
__device__ __forceinline__ void barw() { asm volatile("s_waitcnt lgkmcnt(0)\n\ts_barrier" ::: "memory"); }
__device__ __forceinline__ void barn() { asm volatile("s_barrier" ::: "memory"); }
#define SB() __builtin_amdgcn_sched_barrier(0)
__device__ __forceinline__ u32 pk(float a, float b) {
    f32x2 v = {a, b};
    return __builtin_bit_cast(u32, __builtin_convertvector(v, bf16x2_t));
}
__device__ __forceinline__ float fadd(float a, float b) { float d; asm("v_add_f32 %0, %1, %2" : "=v"(d) : "v"(a), "v"(b)); return d; }
__device__ __forceinline__ float ffma(float a, float b, float c) { float d; asm("v_fma_f32 %0, %1, %2, %3" : "=v"(d) : "v"(a), "v"(b), "v"(c)); return d; }
__device__ __forceinline__ bf16x8 ldsr128(u32 a) { return *(const LAS bf16x8*)a; }
__device__ __forceinline__ s16x4 ldstr(u32 a) { return __builtin_amdgcn_ds_read_tr16_b64_v4i16((LAS s16x4*)a); }
__device__ __forceinline__ bf16x8 cat(s16x4 a, s16x4 b) { return __builtin_shufflevector(a, b, 0, 1, 2, 3, 4, 5, 6, 7); }
#define MFMA(a, b, c) __builtin_amdgcn_mfma_f32_16x16x32_bf16(a, b, c, 0, 0, 0)

__shared__ __attribute__((aligned(1024))) char smem[LDS_BYTES];

struct Stamps {};
#define STAMP(k) do {} while (0)

__device__ __forceinline__ u64 ld_epoch(const Params& p) {
    return __hip_atomic_load((const u64*)((const char*)p.ws + EP_OFF), __ATOMIC_RELAXED, __HIP_MEMORY_SCOPE_AGENT);
}
__device__ __forceinline__ void put_ctl(u64 e) {
    const u32 base = (u32)(uintptr_t)smem;
    *(LAS u64*)(base + CTL_OFF) = e + TAGC;
    *(LAS u32*)(base + CTL_OFF + 8) = 0;
    *(LAS u32x2*)(base + CTL_OFF + 16) = u32x2{0, 0};
}
__device__ __forceinline__ u64 get_tag() { return *(const LAS u64*)((u32)(uintptr_t)smem + CTL_OFF); }
__device__ __forceinline__ void publish(const Params& p, int lane, int fidx, u64 tag) {
    waitv<0>();
    if (lane == 0) {
        const u32 old = __hip_atomic_fetch_add((LAS u32*)((u32)(uintptr_t)smem + CTL_OFF + 8), 1u, __ATOMIC_RELAXED, __HIP_MEMORY_SCOPE_WORKGROUP);
        if (old == 3) __hip_atomic_store((u64*)((char*)p.ws + FL_OFF) + fidx, tag, __ATOMIC_RELAXED, __HIP_MEMORY_SCOPE_AGENT);
    }
}
__device__ __forceinline__ void poll(const u64* f, bool active, u64 tag) {
    for (u32 spins = 0; spins < (1u << 16); ++spins) {
        u64 v = tag;
        if (active) v = __hip_atomic_load(f, __ATOMIC_RELAXED, __HIP_MEMORY_SCOPE_AGENT);
        if (__all(v == tag)) return;
        __builtin_amdgcn_s_sleep(1);
    }
}

__device__ __forceinline__ void wait_ready(bool leader, const u64* f, bool active, u64 tag, u32 word) {
    LAS u32* w = (LAS u32*)((u32)(uintptr_t)smem + CTL_OFF + word);
    if (leader) {
        poll(f, active, tag);
        __hip_atomic_store(w, 1u, __ATOMIC_RELAXED, __HIP_MEMORY_SCOPE_WORKGROUP);
    } else {
        for (u32 spins = 0; spins < (1u << 22); ++spins) {
            if (__builtin_amdgcn_readfirstlane(__hip_atomic_load(w, __ATOMIC_RELAXED, __HIP_MEMORY_SCOPE_WORKGROUP))) break;
            __builtin_amdgcn_s_sleep(1);
        }
    }
    asm volatile("" ::: "memory");
}

constexpr u32 mbslot(int st) { return st % 4 == 3 ? TOP_OFF : (st % 4) * 24576; }
constexpr u32 maslot(int st) { return st % 3 == 2 ? TOP_OFF + 24576 : 73728 + (st % 3) * 8192; }
constexpr u32 MS_OFF = 90112, MZ_OFF = MS_OFF + 3072;
static_assert(MLA <= 20, "slot reuse");
#define MROT 0
constexpr int MAR = 8;
struct MCtx { u32 voffA, voffB[6], wa[4], wb, sca; rsrc_t rq, rv; int rot; };
__device__ __forceinline__ u32 krot(int st, int rot) {
    if (MROT == 0) return st;
    const int k = st + rot;
    return k >= 12 ? k - 12 : k;
}
#define MORD 0
constexpr int mord(int k) {
    constexpr int o0[10] = {0, ~0, 1, ~1, 2, 3, ~2, 4, ~3, 5};
    constexpr int o1[10] = {~0, 0, ~1, 1, ~2, 2, ~3, 3, 4, 5};
    constexpr int o2[10] = {~0, ~1, ~2, ~3, 0, 1, 2, 3, 4, 5};
    return MORD == 0 ? o0[k] : MORD == 1 ? o1[k] : o2[k];
}
template <int P> struct MP {
    static constexpr int ST = P / 10, K = P % 10;
    static constexpr bool isA = mord(K) < 0;
    static constexpr int idx = isA ? ~mord(K) : mord(K);
    static constexpr int ar = (ST * 4 + idx) % MAR;
    static __device__ __forceinline__ void issue(u32x4 (&r)[MAR], const MCtx& c) {
        if constexpr (isA) gld(r[ar], c.voffA, c.rq, krot(ST, c.rot) * 256 + idx * 24576);
        else glds(c.wb + mbslot(ST) + idx * 1024, c.voffB[idx], c.rv, krot(ST, c.rot) * 196608);
    }
    static __device__ __forceinline__ void pro(u32x4 (&r)[MAR], const MCtx& c) {
        issue(r, c);
        if constexpr (P + 1 < MLA) MP<P + 1>::pro(r, c);
    }
    static __device__ __forceinline__ void run(u32x4 (&r)[MAR], const MCtx& c, f32x4& s4, f32x4& z4, Stamps& stp) {
        constexpr int REM = (119 - P) < (MLA - 1) ? (119 - P) : (MLA - 1);
        if constexpr (K == 0) {
            s4 = *(const LAS f32x4*)(c.sca + MS_OFF + krot(ST, c.rot) * 256);
            z4 = *(const LAS f32x4*)(c.sca + MZ_OFF + krot(ST, c.rot) * 256);
        }
        if constexpr (isA) {
            waitv1<REM>(r[ar]);
            const u32x4 q = r[ar];
            u32x2 o;
            o[0] = pk(ffma((float)(int)q[0], s4[0], z4[0]), ffma((float)(int)q[1], s4[1], z4[1]));
            o[1] = pk(ffma((float)(int)q[2], s4[2], z4[2]), ffma((float)(int)q[3], s4[3], z4[3]));
            *(LAS u32x2*)(c.wa[idx] + maslot(ST)) = o;
        } else if constexpr (K == 9) {
            waitv<REM>();
        }
        if constexpr (K == 9) { barw(); STAMP(2 + ST); }
        if constexpr (P + MLA < 120) MP<P + MLA>::issue(r, c);
        if constexpr (P + 1 < 120) MP<P + 1>::run(r, c, s4, z4, stp);
    }
};

struct MSet { bf16x8 a[4]; s16x4 bl[3], bh[3]; };
template <bool DOM, bool DOR>
__device__ __forceinline__ void m_phase(f32x4 (&acc)[4][3], const MSet& m, MSet& r, u32 aa, const u32 (&bb)[3], u32 aoff, u32 boff) {
#pragma unroll
    for (int i = 0; i < 12; ++i) {
        if (DOM) acc[i / 3][i % 3] = MFMA(m.a[i / 3], cat(m.bl[i % 3], m.bh[i % 3]), acc[i / 3][i % 3]);
        if (DOR) {
            if (i == 0) r.bl[0] = ldstr(bb[0] + boff);
            if (i == 1) r.bh[0] = ldstr(bb[0] + boff + 1536);
            if (i == 2) r.a[0] = ldsr128(aa + aoff);
            if (i == 3) r.bl[1] = ldstr(bb[1] + boff);
            if (i == 4) r.bh[1] = ldstr(bb[1] + boff + 1536);
            if (i == 5) r.bl[2] = ldstr(bb[2] + boff);
            if (i == 6) r.bh[2] = ldstr(bb[2] + boff + 1536);
            if (i >= 7 && i < 10) r.a[i - 6] = ldsr128(aa + aoff + (i - 6) * 2048);
        }
        SB();
    }
}

__device__ __forceinline__ void m_prod(const Params& p, int pw, int lane, int et, int gt, int half, Stamps& stp) {
    const u32 base = (u32)(uintptr_t)smem;
    const int q = lane >> 4, c = lane & 15;
    const int kb = half * 768;
    MCtx cx;
    cx.rq = mk(p.qw, D_ * F_ * 4); cx.rv = mk(p.V, F_ * F_ * 2);
    cx.voffA = ((et * 64 + 16 * pw + q) * F_ + kb + 4 * c) * 4;
#pragma unroll
    for (int j = 0; j < 6; ++j) {
        const int P = (6 * pw + j) * 1024 + 16 * lane;
        const int r = P / 384, o = P - r * 384;
        const int sw = ((r >> 1) & 1) | (((r >> 3) & 1) << 1);
        cx.voffB[j] = (kb + r) * (F_ * 2) + gt * 384 + (((o >> 5) ^ sw) << 5) + (o & 16);
    }
#pragma unroll
    for (int i = 0; i < 4; ++i) {
        const int row = 16 * pw + 4 * i + q;
        cx.wa[i] = base + row * 128 + (((c >> 1) ^ ((row >> 1) & 7)) << 4) + (c & 1) * 8;
    }
    cx.wb = base + pw * 6144;
    cx.sca = base + c * 16;
    cx.rot = MROT == 1 ? et % 5 : MROT == 2 ? 2 * (et % 5) : MROT == 3 ? ((et % 5) + 3 * (gt & 3)) % 12 : MROT == 4 ? (gt & 3) : 0;
    u32x4 r[MAR];
    MP<0>::pro(r, cx);
    STAMP(1);
    barn();
    f32x4 s4, z4;
    MP<0>::run(r, cx, s4, z4, stp);
}

__device__ __forceinline__ void m_cons(const Params& p, int wave, int lane, int et, int gt, int half, Stamps& stp) {
    const u32 base = (u32)(uintptr_t)smem;
    const int q = lane >> 4, c = lane & 15;
    const int kb = half * 768;
    {
        const int t = wave * 64 + lane;
        if (t < 192) {
            const u32x2 sv = *(const u32x2*)((const char*)p.sc + (kb + 4 * t) * 2);
            const u32x2 zv = *(const u32x2*)((const char*)p.ze + (kb + 4 * t) * 2);
            f32x4 s4, z4;
            s4[0] = __uint_as_float(sv[0] << 16); s4[1] = __uint_as_float(sv[0] & 0xffff0000u);
            s4[2] = __uint_as_float(sv[1] << 16); s4[3] = __uint_as_float(sv[1] & 0xffff0000u);
            z4[0] = -__uint_as_float(zv[0] << 16) * s4[0]; z4[1] = -__uint_as_float(zv[0] & 0xffff0000u) * s4[1];
            z4[2] = -__uint_as_float(zv[1] << 16) * s4[2]; z4[3] = -__uint_as_float(zv[1] & 0xffff0000u) * s4[3];
            *(LAS f32x4*)(base + MS_OFF + t * 16) = s4;
            *(LAS f32x4*)(base + MZ_OFF + t * 16) = z4;
        } else if (t == 192) {
            put_ctl(ld_epoch(p));
        }
    }
    barw();
    STAMP(1);
    const int qq = c >> 2, pp = c & 3;
    const int sw = (qq >> 1) | ((q & 1) << 1);
    u32 ab[2], bb[3];
    ab[0] = base + c * 128 + ((q ^ ((c >> 1) & 7)) << 4);
    ab[1] = ab[0] ^ 64;
#pragma unroll
    for (int n = 0; n < 3; ++n) bb[n] = base + (8 * q + qq) * 384 + (((3 * wave + n) ^ sw) << 5) + 8 * pp;
    f32x4 acc[4][3] = {};
    MSet s0, s1;
#pragma unroll
    for (int st = 0; st < 12; ++st) {
        SB(); barn(); SB();
        if (st == 0) m_phase<false, true>(acc, s1, s0, ab[0], bb, maslot(st), mbslot(st));
        else m_phase<true, true>(acc, s1, s0, ab[0], bb, maslot(st), mbslot(st));
        m_phase<true, true>(acc, s0, s1, ab[1], bb, maslot(st), mbslot(st) + 12288);
    }
    m_phase<true, false>(acc, s1, s0, 0, bb, 0, 0);
    const rsrc_t rw = mk(p.ws, WS_BYTES);
#pragma unroll
    for (int ebl = 0; ebl < 2; ++ebl)
#pragma unroll
        for (int n = 0; n < 3; ++n) {
            const int nt = 3 * wave + n;
            const int gb = 2 * gt + nt / 6, np = nt % 6;
            u32x4 v;
            v[0] = pk(acc[2 * ebl][n][0], acc[2 * ebl][n][1]);
            v[1] = pk(acc[2 * ebl][n][2], acc[2 * ebl][n][3]);
            v[2] = pk(acc[2 * ebl + 1][n][0], acc[2 * ebl + 1][n][1]);
            v[3] = pk(acc[2 * ebl + 1][n][2], acc[2 * ebl + 1][n][3]);
            const u32 off = MF_OFF + (((half * 16 + gb) * 20 + 2 * et + ebl) * 6 + np) * 1024;
            __builtin_amdgcn_raw_buffer_store_b128(v, rw, lane * 16, off, 16);
        }
}

constexpr u32 xslot(int st) { return st % 4 == 1 ? TOP_OFF : st % 4 == 0 ? 0 : (st % 4 - 1) * 32768; }
struct XCtx { u32 vU[2], vX[2], ldsb; rsrc_t ru, rx; };
template <int ST> struct XP {
    static __device__ __forceinline__ void issue(const XCtx& c) {
#pragma unroll
        for (int j = 0; j < 4; ++j) {
            glds(c.ldsb + xslot(ST) + 16384 + j * 1024, c.vX[j & 1], c.rx, ST * 128 + j * 8 * 1280);
            glds(c.ldsb + xslot(ST) + j * 1024, c.vU[j >> 1], c.ru, ST * 64 * 1280 + j * 4 * 1280);
        }
    }
    static __device__ __forceinline__ void pro(const XCtx& c) {
        issue(c);
        if constexpr (ST + 1 < XA) XP<ST + 1>::pro(c);
    }
    static __device__ __forceinline__ void run(const XCtx& c, Stamps& stp) {
        constexpr int REM = (9 - ST) < (XA - 1) ? (9 - ST) : (XA - 1);
        waitv<REM * 8>();
        barn();
        STAMP(2 + ST);
        if constexpr (ST + XA < 10) XP<ST + XA>::issue(c);
        if constexpr (ST + 1 < 10) XP<ST + 1>::run(c, stp);
    }
};
static_assert(XA <= 2, "slot reuse");

struct XSet { s16x4 al[4], ah[4]; bf16x8 b[4]; };
template <bool DOM, bool DOR>
__device__ __forceinline__ void x_phase(f32x4 (&acc)[4][4], const XSet& m, XSet& r, const u32 (&ab)[4], u32 bb, u32 aoff, u32 boff) {
#pragma unroll
    for (int i = 0; i < 16; ++i) {
        if (DOM) acc[i / 4][i % 4] = MFMA(cat(m.al[i / 4], m.ah[i / 4]), m.b[i % 4], acc[i / 4][i % 4]);
        if (DOR) {
            if (i == 0) r.al[0] = ldstr(ab[0] + aoff);
            if (i == 1) r.ah[0] = ldstr(ab[0] + aoff + 1024);
            if (i >= 2 && i < 6) r.b[i - 2] = ldsr128(bb + boff + (i - 2) * 2048);
            if (i >= 6 && i < 12 && !(i & 1)) r.al[(i - 4) / 2] = ldstr(ab[(i - 4) / 2] + aoff);
            if (i >= 6 && i < 12 && (i & 1)) r.ah[(i - 4) / 2] = ldstr(ab[(i - 4) / 2] + aoff + 1024);
        }
        SB();
    }
}

__device__ __forceinline__ void x_prod(const Params& p, int pw, int lane, int sb, int eblk, Stamps& stp) {
    const u32 base = (u32)(uintptr_t)smem;
    const int q = lane >> 4, c = lane & 15;
    XCtx cx;
    cx.ru = mk(p.U, D_ * D_ * 2); cx.rx = mk(p.x, S_ * D_ * 2);
#pragma unroll
    for (int h = 0; h < 2; ++h) {
        cx.vU[h] = (16 * pw + q) * 1280 + eblk * 256 + (((c >> 1) ^ (q | (h << 2))) << 5) + (c & 1) * 16;
        cx.vX[h] = (sb * 128 + 32 * pw + (lane >> 3)) * 1280 + (((lane & 7) ^ ((4 * h + q) & 7)) << 4);
    }
    cx.ldsb = base + pw * 4096;
    XP<0>::pro(cx);
    STAMP(1);
    XP<0>::run(cx, stp);
}

__device__ __forceinline__ void x_cons(const Params& p, int wave, int lane, int sb, int eblk, Stamps& stp) {
    const u32 base = (u32)(uintptr_t)smem;
    const int q = lane >> 4, c = lane & 15;
    const int we = wave & 1, ws = wave >> 1;
    const int qq = c >> 2, pp = c & 3;
    const int sw = qq | ((q & 1) << 2);
    u64 ep = 0;
    if (wave == 0 && lane == 0) ep = ld_epoch(p);
    u32 ab[4], bb[2];
#pragma unroll
    for (int t = 0; t < 4; ++t) ab[t] = base + (8 * q + qq) * 256 + (((4 * we + t) ^ sw) << 5) + 8 * pp;
    bb[0] = base + 16384 + (64 * ws + c) * 128 + ((q ^ ((c >> 1) & 7)) << 4);
    bb[1] = bb[0] ^ 64;
    f32x4 acc[4][4] = {};
    XSet s0, s1;
#pragma unroll
    for (int st = 0; st < 10; ++st) {
        if (st == 2 && wave == 0 && lane == 0) put_ctl(ep);
        SB(); barn(); SB();
        if (st == 0) x_phase<false, true>(acc, s1, s0, ab, bb[0], xslot(st), xslot(st));
        else x_phase<true, true>(acc, s1, s0, ab, bb[0], xslot(st), xslot(st));
        x_phase<true, true>(acc, s0, s1, ab, bb[1], xslot(st) + 8192, xslot(st));
    }
    x_phase<true, false>(acc, s1, s0, ab, 0, 0, 0);
    const rsrc_t rw = mk(p.ws, WS_BYTES);
#pragma unroll
    for (int ebl = 0; ebl < 2; ++ebl)
#pragma unroll
        for (int u = 0; u < 4; ++u) {
            u32x4 v;
            v[0] = pk(acc[2 * ebl][u][0], acc[2 * ebl][u][1]);
            v[1] = pk(acc[2 * ebl][u][2], acc[2 * ebl][u][3]);
            v[2] = pk(acc[2 * ebl + 1][u][0], acc[2 * ebl + 1][u][1]);
            v[3] = pk(acc[2 * ebl + 1][u][2], acc[2 * ebl + 1][u][3]);
            const u32 off = XF_OFF + ((sb * 20 + 4 * eblk + 2 * we + ebl) * 8 + 4 * ws + u) * 1024;
            __builtin_amdgcn_raw_buffer_store_b128(v, rw, lane * 16, off, 16);
        }
}

constexpr u32 YQ = 1552, YEB = 4 * YQ;
__device__ __forceinline__ u32 add2(u32 a, u32 b) {
    const float lo = fadd(__uint_as_float(a << 16), __uint_as_float(b << 16));
    const float hi = fadd(__uint_as_float(a & 0xffff0000u), __uint_as_float(b & 0xffff0000u));
    return pk(lo, hi);
}
struct YCtx { u32 voff, wa[3]; rsrc_t rw; };
template <int P> struct YP {
    static constexpr int ST = P / 3, J = P % 3;
    static __device__ __forceinline__ void issue(u32x4 (&r)[YLA][2], const YCtx& c) {
        gld_ho(r[P % YLA][0], c.voff, c.rw, (ST * 12 + J) * 1024);
        gld_ho(r[P % YLA][1], c.voff, c.rw, (ST * 12 + J) * 1024 + 16 * 120 * 1024);
    }
    static __device__ __forceinline__ void pro(u32x4 (&r)[YLA][2], const YCtx& c) {
        issue(r, c);
        if constexpr (P + 1 < YLA) YP<P + 1>::pro(r, c);
    }
    static __device__ __forceinline__ void run(u32x4 (&r)[YLA][2], const YCtx& c, Stamps& stp) {
        constexpr int REM = (29 - P) < (YLA - 1) ? (29 - P) : (YLA - 1);
        waitv2<REM * 2>(r[P % YLA][0], r[P % YLA][1]);
        u32x4 o;
#pragma unroll
        for (int e = 0; e < 4; ++e) o[e] = add2(r[P % YLA][0][e], r[P % YLA][1][e]);
        *(LAS u32x4*)(c.wa[J] + ST * 2 * YEB) = o;
        if constexpr (P + YLA < 30) YP<P + YLA>::issue(r, c);
        if constexpr (J == 2) { barw(); STAMP(20 + ST); }
        if constexpr (P + 1 < 30) YP<P + 1>::run(r, c, stp);
    }
};

__device__ __forceinline__ void y_load(const Params& p, int pw, int lane, int gb, u64 tag, bool updater, Stamps& stp) {
    const u32 base = (u32)(uintptr_t)smem;
    const int q = lane >> 4, c = lane & 15;
    YCtx cx;
    cx.rw = mk(p.ws, WS_BYTES);
    cx.voff = MF_OFF + (gb * 120 + 3 * pw) * 1024 + lane * 16;
#pragma unroll
    for (int j = 0; j < 3; ++j) {
        const int ci = 3 * pw + j;
        cx.wa[j] = base + (ci / 6) * YEB + q * YQ + (16 * (ci % 6) + c) * 16;
    }
    const u64* fl = (const u64*)((const char*)p.ws + FL_OFF);
    wait_ready(pw == 0, fl + (gb >> 1) * 20 + lane, lane < 20, tag, 16);
    STAMP(19);
    u32x4 r[YLA][2];
    YP<0>::pro(r, cx);
    YP<0>::run(r, cx, stp);
    if (updater && pw == 0) {
#pragma unroll 1
        for (u32 spins = 0; spins < (1u << 16); ++spins) {
            u32x4 f0, f1;
            gld_ho(f0, FL_OFF + lane * 32, cx.rw, 0);
            gld_ho(f1, FL_OFF + lane * 32 + 16, cx.rw, 0);
            waitv2<0>(f0, f1);
            const u32 tl = (u32)tag, th = (u32)(tag >> 32);
            const bool ok = f0[0] == tl && f0[1] == th && f0[2] == tl && f0[3] == th && f1[0] == tl && f1[1] == th && f1[2] == tl && f1[3] == th;
            if (__all(ok)) break;
            __builtin_amdgcn_s_sleep(1);
        }
        if (lane == 0) __hip_atomic_store((u64*)((char*)p.ws + EP_OFF), tag, __ATOMIC_RELAXED, __HIP_MEMORY_SCOPE_AGENT);
    }
}

__device__ __forceinline__ void y_mma(const Params& p, int w, int lane, int sb, int gb, u64 tag, Stamps& stp) {
    const u32 base = (u32)(uintptr_t)smem;
    const int q = lane >> 4, c = lane & 15;
    const rsrc_t rw = mk(p.ws, WS_BYTES);
    const u64* fl = (const u64*)((const char*)p.ws + FL_OFF);
    wait_ready(w == 0, fl + 160 + sb * 5 + lane, lane < 5, tag, 20);
    STAMP(18);
    bf16x8 xa[20][2];
#pragma unroll
    for (int eb = 0; eb < 20; ++eb) {
#pragma unroll
        for (int u = 0; u < 2; ++u)
            xa[eb][u] = __builtin_bit_cast(bf16x8, __builtin_amdgcn_raw_buffer_load_b128(rw, XF_OFF + (sb * 160 + 2 * w) * 1024 + lane * 16, (eb * 8 + u) * 1024, 16));
        if (2 * eb + 2 > XTH) waitv<XTH>();
    }
    STAMP(19);
    const u32 bb = base + q * YQ + c * 96;
    f32x4 acc[2][6] = {};
    bf16x8 b[YR];
    SB(); barn(); SB();
#pragma unroll
    for (int f = 0; f < YR; ++f) b[f] = ldsr128(bb + (f / 6) * YEB + (f % 6) * 16);
    SB();
#pragma unroll
    for (int f = 0; f < 120; ++f) {
        const int eb = f / 6, n = f % 6;
        acc[0][n] = MFMA(xa[eb][0], b[f % YR], acc[0][n]);
        acc[1][n] = MFMA(xa[eb][1], b[f % YR], acc[1][n]);
        SB();
        if (f + YR < 120) {
            if ((f + YR) % 12 == 0) { barn(); SB(); }
            b[f % YR] = ldsr128(bb + ((f + YR) / 6) * YEB + ((f + YR) % 6) * 16);
            SB();
        }
    }
    STAMP(30);
    const rsrc_t ry = mk(p.y, S_ * F_ * 2);
    const u32 yo = ((sb * 128 + 32 * w + 4 * q) * F_ + gb * 96 + 6 * c) * 2;
#pragma unroll
    for (int u = 0; u < 2; ++u)
#pragma unroll
        for (int r = 0; r < 4; ++r) {
            u32x3 v;
            v[0] = pk(acc[u][0][r], acc[u][1][r]);
            v[1] = pk(acc[u][2][r], acc[u][3][r]);
            v[2] = pk(acc[u][4][r], acc[u][5][r]);
            __builtin_amdgcn_raw_buffer_store_b96(v, ry, yo, (16 * u + r) * F_ * 2, 2);
        }
    asm volatile(".Lqk_end:");
}

__global__ __launch_bounds__(512, 2) void quip_kernel(Params p) {
    const int b = blockIdx.x, xcd = b & 7, i = b >> 3;
    const int lane = threadIdx.x & 63;
    const int wave = __builtin_amdgcn_readfirstlane(threadIdx.x >> 6);
    Stamps stp;
    u32 cpf = 0;
    if (wave == CPFW) {
        u64 pc; u32 len;
        asm volatile("s_getpc_b64 %0\n.Lqk_start:\n\ts_mov_b32 %1, .Lqk_end-.Lqk_start" : "=s"(pc), "=s"(len));
        const rsrc_t rc = mk((const void*)pc, len);
        asm volatile("buffer_load_dword %0, %1, %2, 0 offen" : "=v"(cpf) : "v"(((i % CPFN) * 64 + lane) * 128), "s"(rc) : "memory");
    }
    const int sb2 = 8 * (xcd & 1) + (i & 7), gb2 = 4 * (xcd >> 1) + (i >> 3);
    const int half = xcd & 1, et = 5 * ((xcd >> 1) & 1) + i % 5, gt = 4 * (xcd >> 2) + i / 5;
    const int j = i - 20, sb = 2 * xcd + j / 5, eblk = j % 5;
    if (wave >= 4) {
        if (i < 20) m_prod(p, wave - 4, lane, et, gt, half, stp);
        else if (i < 30) x_prod(p, wave - 4, lane, sb, eblk, stp);
        else barn();
        STAMP(15);
        y_mma(p, wave - 4, lane, sb2, gb2, get_tag(), stp);
    } else {
        int fidx;
        if (i < 20) { m_cons(p, wave, lane, et, gt, half, stp); fidx = gt * 20 + et * 2 + half; }
        else if (i < 30) { x_cons(p, wave, lane, sb, eblk, stp); fidx = 160 + sb * 5 + eblk; }
        else {
            if (wave == 0 && lane == 0) put_ctl(ld_epoch(p));
            barw();
            fidx = b;
        }
        STAMP(15);
        const u64 tag = get_tag();
        publish(p, lane, fidx, tag);
        asm volatile("" :: "v"(cpf));
        STAMP(17);
        y_load(p, wave, lane, gb2, tag, b == 255, stp);
    }
}

extern "C" void kernel_launch(void* const* d_in, const int* in_sizes, int n_in, void* d_out, int out_size, void* d_ws,
                              size_t ws_size, hipStream_t stream) {
    Params p;
    p.x = d_in[0]; p.qw = d_in[1]; p.sc = d_in[2]; p.ze = d_in[3]; p.U = d_in[4]; p.V = d_in[5];
    p.y = d_out; p.ws = d_ws;
    quip_kernel<<<256, 512, 0, stream>>>(p);
}
```

```cpp
#include <hip/hip_runtime.h>
#include <stdint.h>
#pragma clang diagnostic ignored "-Wint-to-pointer-cast"

#define MLA 20
#define XA 2
#define YLA 8
#define YR 6
#define XTH 8

#define CPFW 3
#define CPFN 7
typedef unsigned u32;
typedef unsigned long long u64;
using bf16x8 = __attribute__((ext_vector_type(8))) short;
using s16x4 = __attribute__((ext_vector_type(4))) short;
using f32x4 = __attribute__((ext_vector_type(4))) float;
using u32x4 = __attribute__((ext_vector_type(4))) unsigned;
using u32x3 = __attribute__((ext_vector_type(3))) unsigned;
using u32x2 = __attribute__((ext_vector_type(2))) unsigned;
using bf16x2_t = __attribute__((ext_vector_type(2))) __bf16;
using f32x2 = __attribute__((ext_vector_type(2))) float;
#define LAS __attribute__((address_space(3)))
typedef __amdgpu_buffer_rsrc_t rsrc_t;

constexpr int S_ = 2048, D_ = 640, F_ = 1536;
constexpr u32 XF_OFF = 0, XF_BYTES = S_ * D_ * 2;
constexpr u32 MF_OFF = XF_BYTES, MF_BYTES = 2 * D_ * F_ * 2;
constexpr u32 FL_OFF = MF_OFF + MF_BYTES;
constexpr u32 EP_OFF = FL_OFF + 4096;
constexpr u32 WS_BYTES = EP_OFF + 256;
constexpr int LDS_BYTES = 163840;
constexpr u32 CTL_OFF = 162816;
constexpr u32 TOP_OFF = 129024;
constexpr u64 TAGC = 0x9E3779B97F4A7C15ull;

struct Params {
    const void *x, *qw, *sc, *ze, *U, *V;
    void *y, *ws;
};

__device__ __forceinline__ rsrc_t mk(const void* p, u32 bytes) { return __builtin_amdgcn_make_buffer_rsrc((void*)p, (short)0, (int)bytes, 0x00020000); }
__device__ __forceinline__ void glds(u32 m0v, u32 voff, rsrc_t r, u32 soff) {
    asm volatile("s_mov_b32 m0, %0\n\ts_nop 0\n\tbuffer_load_dwordx4 %1, %2, %3 offen lds" ::"s"(m0v), "v"(voff), "s"(r), "s"(soff) : "memory");
}
__device__ __forceinline__ void gld(u32x4& d, u32 voff, rsrc_t r, u32 soff) {
    asm volatile("buffer_load_dwordx4 %0, %1, %2, %3 offen" : "=v"(d) : "v"(voff), "s"(r), "s"(soff) : "memory");
}
__device__ __forceinline__ void gld_ho(u32x4& d, u32 voff, rsrc_t r, u32 soff) {
    asm volatile("buffer_load_dwordx4 %0, %1, %2, %3 offen sc1" : "=v"(d) : "v"(voff), "s"(r), "s"(soff) : "memory");
}
__device__ __forceinline__ void gld_ho2(u32x2& d, u32 voff, rsrc_t r, u32 soff) {
    asm volatile("buffer_load_dwordx2 %0, %1, %2, %3 offen sc1" : "=v"(d) : "v"(voff), "s"(r), "s"(soff) : "memory");
}
template <int N> __device__ __forceinline__ void waitv() { asm volatile("s_waitcnt vmcnt(%0)" ::"n"(N) : "memory"); }
template <int N, class T> __device__ __forceinline__ void waitv1(T& a) { asm volatile("s_waitcnt vmcnt(%1)" : "+v"(a) : "n"(N) : "memory"); }
template <int N, class T> __device__ __forceinline__ void waitv2(T& a, T& b) { asm volatile("s_waitcnt vmcnt(%2)" : "+v"(a), "+v"(b) : "n"(N) : "memory"); }
__device__ __forceinline__ void barw() { asm volatile("s_waitcnt lgkmcnt(0)\n\ts_barrier" ::: "memory"); }
__device__ __forceinline__ void barn() { asm volatile("s_barrier" ::: "memory"); }
#define SB() __builtin_amdgcn_sched_barrier(0)
__device__ __forceinline__ u32 pk(float a, float b) {
    f32x2 v = {a, b};
    return __builtin_bit_cast(u32, __builtin_convertvector(v, bf16x2_t));
}
__device__ __forceinline__ float fadd(float a, float b) { float d; asm("v_add_f32 %0, %1, %2" : "=v"(d) : "v"(a), "v"(b)); return d; }
__device__ __forceinline__ float ffma(float a, float b, float c) { float d; asm("v_fma_f32 %0, %1, %2, %3" : "=v"(d) : "v"(a), "v"(b), "v"(c)); return d; }
__device__ __forceinline__ bf16x8 ldsr128(u32 a) { return *(const LAS bf16x8*)a; }
__device__ __forceinline__ s16x4 ldstr(u32 a) { return __builtin_amdgcn_ds_read_tr16_b64_v4i16((LAS s16x4*)a); }
__device__ __forceinline__ bf16x8 cat(s16x4 a, s16x4 b) { return __builtin_shufflevector(a, b, 0, 1, 2, 3, 4, 5, 6, 7); }
#define MFMA(a, b, c) __builtin_amdgcn_mfma_f32_16x16x32_bf16(a, b, c, 0, 0, 0)

__shared__ __attribute__((aligned(1024))) char smem[LDS_BYTES];

struct Stamps {};
#define STAMP(k) do {} while (0)

__device__ __forceinline__ u64 ld_epoch(const Params& p) {
    return __hip_atomic_load((const u64*)((const char*)p.ws + EP_OFF), __ATOMIC_RELAXED, __HIP_MEMORY_SCOPE_AGENT);
}
__device__ __forceinline__ void put_ctl(u64 e) {
    const u32 base = (u32)(uintptr_t)smem;
    *(LAS u64*)(base + CTL_OFF) = e + TAGC;
    *(LAS u32*)(base + CTL_OFF + 8) = 0;
    *(LAS u32x2*)(base + CTL_OFF + 16) = u32x2{0, 0};
}
__device__ __forceinline__ u64 get_tag() { return *(const LAS u64*)((u32)(uintptr_t)smem + CTL_OFF); }
__device__ __forceinline__ void publish(const Params& p, int lane, int fidx, u64 tag) {
    waitv<0>();
    if (lane == 0) {
        const u32 old = __hip_atomic_fetch_add((LAS u32*)((u32)(uintptr_t)smem + CTL_OFF + 8), 1u, __ATOMIC_RELAXED, __HIP_MEMORY_SCOPE_WORKGROUP);
        if (old == 3) __hip_atomic_store((u64*)((char*)p.ws + FL_OFF) + fidx, tag, __ATOMIC_RELAXED, __HIP_MEMORY_SCOPE_AGENT);
    }
}
__device__ __forceinline__ void poll(const u64* f, bool active, u64 tag) {
    for (u32 spins = 0; spins < (1u << 16); ++spins) {
        u64 v = tag;
        if (active) v = __hip_atomic_load(f, __ATOMIC_RELAXED, __HIP_MEMORY_SCOPE_AGENT);
        if (__all(v == tag)) return;
        __builtin_amdgcn_s_sleep(1);
    }
}

__device__ __forceinline__ void wait_ready(bool leader, const u64* f, bool active, u64 tag, u32 word) {
    LAS u32* w = (LAS u32*)((u32)(uintptr_t)smem + CTL_OFF + word);
    if (leader) {
        poll(f, active, tag);
        __hip_atomic_store(w, 1u, __ATOMIC_RELAXED, __HIP_MEMORY_SCOPE_WORKGROUP);
    } else {
        for (u32 spins = 0; spins < (1u << 22); ++spins) {
            if (__builtin_amdgcn_readfirstlane(__hip_atomic_load(w, __ATOMIC_RELAXED, __HIP_MEMORY_SCOPE_WORKGROUP))) break;
            __builtin_amdgcn_s_sleep(1);
        }
    }
    asm volatile("" ::: "memory");
}

constexpr u32 mbslot(int st) { return st % 4 == 3 ? TOP_OFF : (st % 4) * 24576; }
constexpr u32 maslot(int st) { return st % 3 == 2 ? TOP_OFF + 24576 : 73728 + (st % 3) * 8192; }
constexpr u32 MS_OFF = 90112, MZ_OFF = MS_OFF + 3072;
static_assert(MLA <= 20, "slot reuse");
#define MROT 3
constexpr int MAR = 8;
struct MCtx { u32 voffA, voffB[6], wa[4], wb, sca; rsrc_t rq, rv; int rot; };
__device__ __forceinline__ u32 krot(int st, int rot) {
    if (MROT == 0) return st;
    const int k = st + rot;
    return k >= 12 ? k - 12 : k;
}
#define MORD 0
constexpr int mord(int k) {
    constexpr int o0[10] = {0, ~0, 1, ~1, 2, 3, ~2, 4, ~3, 5};
    constexpr int o1[10] = {~0, 0, ~1, 1, ~2, 2, ~3, 3, 4, 5};
    constexpr int o2[10] = {~0, ~1, ~2, ~3, 0, 1, 2, 3, 4, 5};
    return MORD == 0 ? o0[k] : MORD == 1 ? o1[k] : o2[k];
}
template <int P> struct MP {
    static constexpr int ST = P / 10, K = P % 10;
    static constexpr bool isA = mord(K) < 0;
    static constexpr int idx = isA ? ~mord(K) : mord(K);
    static constexpr int ar = (ST * 4 + idx) % MAR;
    static __device__ __forceinline__ void issue(u32x4 (&r)[MAR], const MCtx& c) {
        if constexpr (isA) gld(r[ar], c.voffA, c.rq, krot(ST, c.rot) * 256 + idx * 24576);
        else glds(c.wb + mbslot(ST) + idx * 1024, c.voffB[idx], c.rv, krot(ST, c.rot) * 196608);
    }
    static __device__ __forceinline__ void pro(u32x4 (&r)[MAR], const MCtx& c) {
        issue(r, c);
        if constexpr (P + 1 < MLA) MP<P + 1>::pro(r, c);
    }
    static __device__ __forceinline__ void run(u32x4 (&r)[MAR], const MCtx& c, f32x4& s4, f32x4& z4, Stamps& stp) {
        constexpr int REM = (119 - P) < (MLA - 1) ? (119 - P) : (MLA - 1);
        if constexpr (K == 0) {
            s4 = *(const LAS f32x4*)(c.sca + MS_OFF + krot(ST, c.rot) * 256);
            z4 = *(const LAS f32x4*)(c.sca + MZ_OFF + krot(ST, c.rot) * 256);
        }
        if constexpr (isA) {
            waitv1<REM>(r[ar]);
            const u32x4 q = r[ar];
            u32x2 o;
            o[0] = pk(ffma((float)(int)q[0], s4[0], z4[0]), ffma((float)(int)q[1], s4[1], z4[1]));
            o[1] = pk(ffma((float)(int)q[2], s4[2], z4[2]), ffma((float)(int)q[3], s4[3], z4[3]));
            *(LAS u32x2*)(c.wa[idx] + maslot(ST)) = o;
        } else if constexpr (K == 9) {
            waitv<REM>();
        }
        if constexpr (K == 9) { barw(); STAMP(2 + ST); }
        if constexpr (P + MLA < 120) MP<P + MLA>::issue(r, c);
        if constexpr (P + 1 < 120) MP<P + 1>::run(r, c, s4, z4, stp);
    }
};

struct MSet { bf16x8 a[4]; s16x4 bl[3], bh[3]; };
template <bool DOM, bool DOR>
__device__ __forceinline__ void m_phase(f32x4 (&acc)[4][3], const MSet& m, MSet& r, u32 aa, const u32 (&bb)[3], u32 aoff, u32 boff) {
#pragma unroll
    for (int i = 0; i < 12; ++i) {
        if (DOM) acc[i / 3][i % 3] = MFMA(m.a[i / 3], cat(m.bl[i % 3], m.bh[i % 3]), acc[i / 3][i % 3]);
        if (DOR) {
            if (i == 0) r.bl[0] = ldstr(bb[0] + boff);
            if (i == 1) r.bh[0] = ldstr(bb[0] + boff + 1536);
            if (i == 2) r.a[0] = ldsr128(aa + aoff);
            if (i == 3) r.bl[1] = ldstr(bb[1] + boff);
            if (i == 4) r.bh[1] = ldstr(bb[1] + boff + 1536);
            if (i == 5) r.bl[2] = ldstr(bb[2] + boff);
            if (i == 6) r.bh[2] = ldstr(bb[2] + boff + 1536);
            if (i >= 7 && i < 10) r.a[i - 6] = ldsr128(aa + aoff + (i - 6) * 2048);
        }
        SB();
    }
}

__device__ __forceinline__ void m_prod(const Params& p, int pw, int lane, int et, int gt, int half, Stamps& stp) {
    const u32 base = (u32)(uintptr_t)smem;
    const int q = lane >> 4, c = lane & 15;
    const int kb = half * 768;
    MCtx cx;
    cx.rq = mk(p.qw, D_ * F_ * 4); cx.rv = mk(p.V, F_ * F_ * 2);
    cx.voffA = ((et * 64 + 16 * pw + q) * F_ + kb + 4 * c) * 4;
#pragma unroll
    for (int j = 0; j < 6; ++j) {
        const int P = (6 * pw + j) * 1024 + 16 * lane;
        const int r = P / 384, o = P - r * 384;
        const int sw = ((r >> 1) & 1) | (((r >> 3) & 1) << 1);
        cx.voffB[j] = (kb + r) * (F_ * 2) + gt * 384 + (((o >> 5) ^ sw) << 5) + (o & 16);
    }
#pragma unroll
    for (int i = 0; i < 4; ++i) {
        const int row = 16 * pw + 4 * i + q;
        cx.wa[i] = base + row * 128 + (((c >> 1) ^ ((row >> 1) & 7)) << 4) + (c & 1) * 8;
    }
    cx.wb = base + pw * 6144;
    cx.sca = base + c * 16;
    cx.rot = MROT == 1 ? et % 5 : MROT == 2 ? 2 * (et % 5) : MROT == 3 ? ((et % 5) + 3 * (gt & 3)) % 12 : MROT == 4 ? (gt & 3) : 0;
    u32x4 r[MAR];
    MP<0>::pro(r, cx);
    STAMP(1);
    barn();
    f32x4 s4, z4;
    MP<0>::run(r, cx, s4, z4, stp);
}

__device__ __forceinline__ void m_cons(const Params& p, int wave, int lane, int et, int gt, int half, Stamps& stp) {
    const u32 base = (u32)(uintptr_t)smem;
    const int q = lane >> 4, c = lane & 15;
    const int kb = half * 768;
    {
        const int t = wave * 64 + lane;
        if (t < 192) {
            const u32x2 sv = *(const u32x2*)((const char*)p.sc + (kb + 4 * t) * 2);
            const u32x2 zv = *(const u32x2*)((const char*)p.ze + (kb + 4 * t) * 2);
            f32x4 s4, z4;
            s4[0] = __uint_as_float(sv[0] << 16); s4[1] = __uint_as_float(sv[0] & 0xffff0000u);
            s4[2] = __uint_as_float(sv[1] << 16); s4[3] = __uint_as_float(sv[1] & 0xffff0000u);
            z4[0] = -__uint_as_float(zv[0] << 16) * s4[0]; z4[1] = -__uint_as_float(zv[0] & 0xffff0000u) * s4[1];
            z4[2] = -__uint_as_float(zv[1] << 16) * s4[2]; z4[3] = -__uint_as_float(zv[1] & 0xffff0000u) * s4[3];
            *(LAS f32x4*)(base + MS_OFF + t * 16) = s4;
            *(LAS f32x4*)(base + MZ_OFF + t * 16) = z4;
        } else if (t == 192) {
            put_ctl(ld_epoch(p));
        }
    }
    barw();
    STAMP(1);
    const int qq = c >> 2, pp = c & 3;
    const int sw = (qq >> 1) | ((q & 1) << 1);
    u32 ab[2], bb[3];
    ab[0] = base + c * 128 + ((q ^ ((c >> 1) & 7)) << 4);
    ab[1] = ab[0] ^ 64;
#pragma unroll
    for (int n = 0; n < 3; ++n) bb[n] = base + (8 * q + qq) * 384 + (((3 * wave + n) ^ sw) << 5) + 8 * pp;
    f32x4 acc[4][3] = {};
    MSet s0, s1;
#pragma unroll
    for (int st = 0; st < 12; ++st) {
        SB(); barn(); SB();
        if (st == 0) m_phase<false, true>(acc, s1, s0, ab[0], bb, maslot(st), mbslot(st));
        else m_phase<true, true>(acc, s1, s0, ab[0], bb, maslot(st), mbslot(st));
        m_phase<true, true>(acc, s0, s1, ab[1], bb, maslot(st), mbslot(st) + 12288);
    }
    m_phase<true, false>(acc, s1, s0, 0, bb, 0, 0);
    const rsrc_t rw = mk(p.ws, WS_BYTES);
#pragma unroll
    for (int ebl = 0; ebl < 2; ++ebl)
#pragma unroll
        for (int n = 0; n < 3; ++n) {
            const int nt = 3 * wave + n;
            const int gb = 2 * gt + nt / 6, np = nt % 6;
            u32x4 v;
            v[0] = pk(acc[2 * ebl][n][0], acc[2 * ebl][n][1]);
            v[1] = pk(acc[2 * ebl][n][2], acc[2 * ebl][n][3]);
            v[2] = pk(acc[2 * ebl + 1][n][0], acc[2 * ebl + 1][n][1]);
            v[3] = pk(acc[2 * ebl + 1][n][2], acc[2 * ebl + 1][n][3]);
            const u32 off = MF_OFF + (((half * 16 + gb) * 20 + 2 * et + ebl) * 6 + np) * 1024;
            __builtin_amdgcn_raw_buffer_store_b128(v, rw, lane * 16, off, 16);
        }
}

constexpr u32 xslot(int st) { return st % 4 == 1 ? TOP_OFF : st % 4 == 0 ? 0 : (st % 4 - 1) * 32768; }
#define XROT 0
struct XCtx { u32 vU[2], vX[2], ldsb; rsrc_t ru, rx; int rot; };
template <int ST> struct XP {
    static __device__ __forceinline__ void issue(const XCtx& c) {
        int k = ST;
        if (XROT) { k = ST + c.rot; k = k >= 10 ? k - 10 : k; }
#pragma unroll
        for (int j = 0; j < 4; ++j) {
            glds(c.ldsb + xslot(ST) + 16384 + j * 1024, c.vX[j & 1], c.rx, k * 128 + j * 8 * 1280);
            glds(c.ldsb + xslot(ST) + j * 1024, c.vU[j >> 1], c.ru, k * 64 * 1280 + j * 4 * 1280);
        }
    }
    static __device__ __forceinline__ void pro(const XCtx& c) {
        issue(c);
        if constexpr (ST + 1 < XA) XP<ST + 1>::pro(c);
    }
    static __device__ __forceinline__ void run(const XCtx& c, Stamps& stp) {
        constexpr int REM = (9 - ST) < (XA - 1) ? (9 - ST) : (XA - 1);
        waitv<REM * 8>();
        barn();
        STAMP(2 + ST);
        if constexpr (ST + XA < 10) XP<ST + XA>::issue(c);
        if constexpr (ST + 1 < 10) XP<ST + 1>::run(c, stp);
    }
};
static_assert(XA <= 2, "slot reuse");

struct XSet { s16x4 al[4], ah[4]; bf16x8 b[4]; };
template <bool DOM, bool DOR>
__device__ __forceinline__ void x_phase(f32x4 (&acc)[4][4], const XSet& m, XSet& r, const u32 (&ab)[4], u32 bb, u32 aoff, u32 boff) {
#pragma unroll
    for (int i = 0; i < 16; ++i) {
        if (DOM) acc[i / 4][i % 4] = MFMA(cat(m.al[i / 4], m.ah[i / 4]), m.b[i % 4], acc[i / 4][i % 4]);
        if (DOR) {
            if (i == 0) r.al[0] = ldstr(ab[0] + aoff);
            if (i == 1) r.ah[0] = ldstr(ab[0] + aoff + 1024);
            if (i >= 2 && i < 6) r.b[i - 2] = ldsr128(bb + boff + (i - 2) * 2048);
            if (i >= 6 && i < 12 && !(i & 1)) r.al[(i - 4) / 2] = ldstr(ab[(i - 4) / 2] + aoff);
            if (i >= 6 && i < 12 && (i & 1)) r.ah[(i - 4) / 2] = ldstr(ab[(i - 4) / 2] + aoff + 1024);
        }
        SB();
    }
}

__device__ __forceinline__ void x_prod(const Params& p, int pw, int lane, int sb, int eblk, Stamps& stp) {
    const u32 base = (u32)(uintptr_t)smem;
    const int q = lane >> 4, c = lane & 15;
    XCtx cx;
    cx.ru = mk(p.U, D_ * D_ * 2); cx.rx = mk(p.x, S_ * D_ * 2);
#pragma unroll
    for (int h = 0; h < 2; ++h) {
        cx.vU[h] = (16 * pw + q) * 1280 + eblk * 256 + (((c >> 1) ^ (q | (h << 2))) << 5) + (c & 1) * 16;
        cx.vX[h] = (sb * 128 + 32 * pw + (lane >> 3)) * 1280 + (((lane & 7) ^ ((4 * h + q) & 7)) << 4);
    }
    cx.ldsb = base + pw * 4096;
    cx.rot = XROT == 1 ? (2 * eblk + 5 * (sb & 1)) % 10 : XROT == 2 ? 2 * eblk : (2 * eblk + (sb & 1)) % 10;
    XP<0>::pro(cx);
    STAMP(1);
    XP<0>::run(cx, stp);
}

__device__ __forceinline__ void x_cons(const Params& p, int wave, int lane, int sb, int eblk, Stamps& stp) {
    const u32 base = (u32)(uintptr_t)smem;
    const int q = lane >> 4, c = lane & 15;
    const int we = wave & 1, ws = wave >> 1;
    const int qq = c >> 2, pp = c & 3;
    const int sw = qq | ((q & 1) << 2);
    u64 ep = 0;
    if (wave == 0 && lane == 0) ep = ld_epoch(p);
    u32 ab[4], bb[2];
#pragma unroll
    for (int t = 0; t < 4; ++t) ab[t] = base + (8 * q + qq) * 256 + (((4 * we + t) ^ sw) << 5) + 8 * pp;
    bb[0] = base + 16384 + (64 * ws + c) * 128 + ((q ^ ((c >> 1) & 7)) << 4);
    bb[1] = bb[0] ^ 64;
    f32x4 acc[4][4] = {};
    XSet s0, s1;
#pragma unroll
    for (int st = 0; st < 10; ++st) {
        if (st == 2 && wave == 0 && lane == 0) put_ctl(ep);
        SB(); barn(); SB();
        if (st == 0) x_phase<false, true>(acc, s1, s0, ab, bb[0], xslot(st), xslot(st));
        else x_phase<true, true>(acc, s1, s0, ab, bb[0], xslot(st), xslot(st));
        x_phase<true, true>(acc, s0, s1, ab, bb[1], xslot(st) + 8192, xslot(st));
    }
    x_phase<true, false>(acc, s1, s0, ab, 0, 0, 0);
    const rsrc_t rw = mk(p.ws, WS_BYTES);
#pragma unroll
    for (int ebl = 0; ebl < 2; ++ebl)
#pragma unroll
        for (int u = 0; u < 4; ++u) {
            u32x4 v;
            v[0] = pk(acc[2 * ebl][u][0], acc[2 * ebl][u][1]);
            v[1] = pk(acc[2 * ebl][u][2], acc[2 * ebl][u][3]);
            v[2] = pk(acc[2 * ebl + 1][u][0], acc[2 * ebl + 1][u][1]);
            v[3] = pk(acc[2 * ebl + 1][u][2], acc[2 * ebl + 1][u][3]);
            const u32 off = XF_OFF + ((sb * 20 + 4 * eblk + 2 * we + ebl) * 8 + 4 * ws + u) * 1024;
            __builtin_amdgcn_raw_buffer_store_b128(v, rw, lane * 16, off, 16);
        }
}

constexpr u32 YQ = 1552, YEB = 4 * YQ;
__device__ __forceinline__ u32 add2(u32 a, u32 b) {
    const float lo = fadd(__uint_as_float(a << 16), __uint_as_float(b << 16));
    const float hi = fadd(__uint_as_float(a & 0xffff0000u), __uint_as_float(b & 0xffff0000u));
    return pk(lo, hi);
}
struct YCtx { u32 voff, wa[3]; rsrc_t rw; };
template <int P> struct YP {
    static constexpr int ST = P / 3, J = P % 3;
    static __device__ __forceinline__ void issue(u32x4 (&r)[YLA][2], const YCtx& c) {
        gld_ho(r[P % YLA][0], c.voff, c.rw, (ST * 12 + J) * 1024);
        gld_ho(r[P % YLA][1], c.voff, c.rw, (ST * 12 + J) * 1024 + 16 * 120 * 1024);
    }
    static __device__ __forceinline__ void pro(u32x4 (&r)[YLA][2], const YCtx& c) {
        issue(r, c);
        if constexpr (P + 1 < YLA) YP<P + 1>::pro(r, c);
    }
    static __device__ __forceinline__ void run(u32x4 (&r)[YLA][2], const YCtx& c, Stamps& stp) {
        constexpr int REM = (29 - P) < (YLA - 1) ? (29 - P) : (YLA - 1);
        waitv2<REM * 2>(r[P % YLA][0], r[P % YLA][1]);
        u32x4 o;
#pragma unroll
        for (int e = 0; e < 4; ++e) o[e] = add2(r[P % YLA][0][e], r[P % YLA][1][e]);
        *(LAS u32x4*)(c.wa[J] + ST * 2 * YEB) = o;
        if constexpr (P + YLA < 30) YP<P + YLA>::issue(r, c);
        if constexpr (J == 2) { barw(); STAMP(20 + ST); }
        if constexpr (P + 1 < 30) YP<P + 1>::run(r, c, stp);
    }
};

__device__ __forceinline__ void y_load(const Params& p, int pw, int lane, int gb, u64 tag, bool updater, Stamps& stp) {
    const u32 base = (u32)(uintptr_t)smem;
    const int q = lane >> 4, c = lane & 15;
    YCtx cx;
    cx.rw = mk(p.ws, WS_BYTES);
    cx.voff = MF_OFF + (gb * 120 + 3 * pw) * 1024 + lane * 16;
#pragma unroll
    for (int j = 0; j < 3; ++j) {
        const int ci = 3 * pw + j;
        cx.wa[j] = base + (ci / 6) * YEB + q * YQ + (16 * (ci % 6) + c) * 16;
    }
    const u64* fl = (const u64*)((const char*)p.ws + FL_OFF);
    wait_ready(pw == 0, fl + (gb >> 1) * 20 + lane, lane < 20, tag, 16);
    STAMP(19);
    u32x4 r[YLA][2];
    YP<0>::pro(r, cx);
    YP<0>::run(r, cx, stp);
    if (updater && pw == 0) {
#pragma unroll 1
        for (u32 spins = 0; spins < (1u << 16); ++spins) {
            u32x4 f0, f1;
            gld_ho(f0, FL_OFF + lane * 32, cx.rw, 0);
            gld_ho(f1, FL_OFF + lane * 32 + 16, cx.rw, 0);
            waitv2<0>(f0, f1);
            const u32 tl = (u32)tag, th = (u32)(tag >> 32);
            const bool ok = f0[0] == tl && f0[1] == th && f0[2] == tl && f0[3] == th && f1[0] == tl && f1[1] == th && f1[2] == tl && f1[3] == th;
            if (__all(ok)) break;
            __builtin_amdgcn_s_sleep(1);
        }
        if (lane == 0) __hip_atomic_store((u64*)((char*)p.ws + EP_OFF), tag, __ATOMIC_RELAXED, __HIP_MEMORY_SCOPE_AGENT);
    }
}

__device__ __forceinline__ void y_mma(const Params& p, int w, int lane, int sb, int gb, u64 tag, Stamps& stp) {
    const u32 base = (u32)(uintptr_t)smem;
    const int q = lane >> 4, c = lane & 15;
    const rsrc_t rw = mk(p.ws, WS_BYTES);
    const u64* fl = (const u64*)((const char*)p.ws + FL_OFF);
    wait_ready(w == 0, fl + 160 + sb * 5 + lane, lane < 5, tag, 20);
    STAMP(18);
    bf16x8 xa[20][2];
#pragma unroll
    for (int eb = 0; eb < 20; ++eb) {
#pragma unroll
        for (int u = 0; u < 2; ++u)
            xa[eb][u] = __builtin_bit_cast(bf16x8, __builtin_amdgcn_raw_buffer_load_b128(rw, XF_OFF + (sb * 160 + 2 * w) * 1024 + lane * 16, (eb * 8 + u) * 1024, 16));
        if (2 * eb + 2 > XTH) waitv<XTH>();
    }
    STAMP(19);
    const u32 bb = base + q * YQ + c * 96;
    f32x4 acc[2][6] = {};
    bf16x8 b[YR];
    SB(); barn(); SB();
#pragma unroll
    for (int f = 0; f < YR; ++f) b[f] = ldsr128(bb + (f / 6) * YEB + (f % 6) * 16);
    SB();
#pragma unroll
    for (int f = 0; f < 120; ++f) {
        const int eb = f / 6, n = f % 6;
        acc[0][n] = MFMA(xa[eb][0], b[f % YR], acc[0][n]);
        acc[1][n] = MFMA(xa[eb][1], b[f % YR], acc[1][n]);
        SB();
        if (f + YR < 120) {
            if ((f + YR) % 12 == 0) { barn(); SB(); }
            b[f % YR] = ldsr128(bb + ((f + YR) / 6) * YEB + ((f + YR) % 6) * 16);
            SB();
        }
    }
    STAMP(30);
    const rsrc_t ry = mk(p.y, S_ * F_ * 2);
    const u32 yo = ((sb * 128 + 32 * w + 4 * q) * F_ + gb * 96 + 6 * c) * 2;
#pragma unroll
    for (int u = 0; u < 2; ++u)
#pragma unroll
        for (int r = 0; r < 4; ++r) {
            u32x3 v;
            v[0] = pk(acc[u][0][r], acc[u][1][r]);
            v[1] = pk(acc[u][2][r], acc[u][3][r]);
            v[2] = pk(acc[u][4][r], acc[u][5][r]);
            __builtin_amdgcn_raw_buffer_store_b96(v, ry, yo, (16 * u + r) * F_ * 2, 2);
        }
    asm volatile(".Lqk_end:");
}

__global__ __launch_bounds__(512, 2) void quip_kernel(Params p) {
    const int b = blockIdx.x, xcd = b & 7, i = b >> 3;
    const int lane = threadIdx.x & 63;
    const int wave = __builtin_amdgcn_readfirstlane(threadIdx.x >> 6);
    Stamps stp;
    u32 cpf = 0;
    if (wave == CPFW) {
        u64 pc; u32 len;
        asm volatile("s_getpc_b64 %0\n.Lqk_start:\n\ts_mov_b32 %1, .Lqk_end-.Lqk_start" : "=s"(pc), "=s"(len));
        const rsrc_t rc = mk((const void*)pc, len);
        asm volatile("buffer_load_dword %0, %1, %2, 0 offen" : "=v"(cpf) : "v"(((i % CPFN) * 64 + lane) * 128), "s"(rc) : "memory");
    }
    const int sb2 = 8 * (xcd & 1) + (i & 7), gb2 = 4 * (xcd >> 1) + (i >> 3);
    const int half = xcd & 1, et = 5 * ((xcd >> 1) & 1) + i % 5, gt = 4 * (xcd >> 2) + i / 5;
    const int j = i - 20, sb = 2 * xcd + j / 5, eblk = j % 5;
    if (wave >= 4) {
        if (i < 20) m_prod(p, wave - 4, lane, et, gt, half, stp);
        else if (i < 30) x_prod(p, wave - 4, lane, sb, eblk, stp);
        else barn();
        STAMP(15);
        y_mma(p, wave - 4, lane, sb2, gb2, get_tag(), stp);
    } else {
        int fidx;
        if (i < 20) { m_cons(p, wave, lane, et, gt, half, stp); fidx = gt * 20 + et * 2 + half; }
        else if (i < 30) { x_cons(p, wave, lane, sb, eblk, stp); fidx = 160 + sb * 5 + eblk; }
        else {
            if (wave == 0 && lane == 0) put_ctl(ld_epoch(p));
            barw();
            fidx = b;
        }
        STAMP(15);
        const u64 tag = get_tag();
        publish(p, lane, fidx, tag);
        asm volatile("" :: "v"(cpf));
        STAMP(17);
        y_load(p, wave, lane, gb2, tag, b == 255, stp);
    }
}

extern "C" void kernel_launch(void* const* d_in, const int* in_sizes, int n_in, void* d_out, int out_size, void* d_ws,
                              size_t ws_size, hipStream_t stream) {
    Params p;
    p.x = d_in[0]; p.qw = d_in[1]; p.sc = d_in[2]; p.ze = d_in[3]; p.U = d_in[4]; p.V = d_in[5];
    p.y = d_out; p.ws = d_ws;
    quip_kernel<<<256, 512, 0, stream>>>(p);
}
```

```cpp
#define PROF 1
#define AMP_SEL -1
#define AMP_B 0
#define AMP_W 4
#include <hip/hip_runtime.h>
#include <stdint.h>
#pragma clang diagnostic ignored "-Wint-to-pointer-cast"

#define MLA 20
#define XA 2
#define YLA 8
#define YR 6
#define XTH 8

#define CPFW 3
#define CPFN 7
typedef unsigned u32;
typedef unsigned long long u64;
using bf16x8 = __attribute__((ext_vector_type(8))) short;
using s16x4 = __attribute__((ext_vector_type(4))) short;
using f32x4 = __attribute__((ext_vector_type(4))) float;
using u32x4 = __attribute__((ext_vector_type(4))) unsigned;
using u32x3 = __attribute__((ext_vector_type(3))) unsigned;
using u32x2 = __attribute__((ext_vector_type(2))) unsigned;
using bf16x2_t = __attribute__((ext_vector_type(2))) __bf16;
using f32x2 = __attribute__((ext_vector_type(2))) float;
#define LAS __attribute__((address_space(3)))
typedef __amdgpu_buffer_rsrc_t rsrc_t;

constexpr int S_ = 2048, D_ = 640, F_ = 1536;
constexpr u32 XF_OFF = 0, XF_BYTES = S_ * D_ * 2;
constexpr u32 MF_OFF = XF_BYTES, MF_BYTES = 2 * D_ * F_ * 2;
constexpr u32 FL_OFF = MF_OFF + MF_BYTES;
constexpr u32 EP_OFF = FL_OFF + 4096;
constexpr u32 WS_BYTES = EP_OFF + 256;
constexpr int LDS_BYTES = 163840;
constexpr u32 CTL_OFF = 162816;
constexpr u32 TOP_OFF = 129024;
constexpr u64 TAGC = 0x9E3779B97F4A7C15ull;

struct Params {
    const void *x, *qw, *sc, *ze, *U, *V;
    void *y, *ws;
};

__device__ __forceinline__ rsrc_t mk(const void* p, u32 bytes) { return __builtin_amdgcn_make_buffer_rsrc((void*)p, (short)0, (int)bytes, 0x00020000); }
__device__ __forceinline__ void glds(u32 m0v, u32 voff, rsrc_t r, u32 soff) {
    asm volatile("s_mov_b32 m0, %0\n\ts_nop 0\n\tbuffer_load_dwordx4 %1, %2, %3 offen lds" ::"s"(m0v), "v"(voff), "s"(r), "s"(soff) : "memory");
}
__device__ __forceinline__ void gld(u32x4& d, u32 voff, rsrc_t r, u32 soff) {
    asm volatile("buffer_load_dwordx4 %0, %1, %2, %3 offen" : "=v"(d) : "v"(voff), "s"(r), "s"(soff) : "memory");
}
__device__ __forceinline__ void gld_ho(u32x4& d, u32 voff, rsrc_t r, u32 soff) {
    asm volatile("buffer_load_dwordx4 %0, %1, %2, %3 offen sc1" : "=v"(d) : "v"(voff), "s"(r), "s"(soff) : "memory");
}
__device__ __forceinline__ void gld_ho2(u32x2& d, u32 voff, rsrc_t r, u32 soff) {
    asm volatile("buffer_load_dwordx2 %0, %1, %2, %3 offen sc1" : "=v"(d) : "v"(voff), "s"(r), "s"(soff) : "memory");
}
template <int N> __device__ __forceinline__ void waitv() { asm volatile("s_waitcnt vmcnt(%0)" ::"n"(N) : "memory"); }
template <int N, class T> __device__ __forceinline__ void waitv1(T& a) { asm volatile("s_waitcnt vmcnt(%1)" : "+v"(a) : "n"(N) : "memory"); }
template <int N, class T> __device__ __forceinline__ void waitv2(T& a, T& b) { asm volatile("s_waitcnt vmcnt(%2)" : "+v"(a), "+v"(b) : "n"(N) : "memory"); }
__device__ __forceinline__ void barw() { asm volatile("s_waitcnt lgkmcnt(0)\n\ts_barrier" ::: "memory"); }
__device__ __forceinline__ void barn() { asm volatile("s_barrier" ::: "memory"); }
#define SB() __builtin_amdgcn_sched_barrier(0)
__device__ __forceinline__ u32 pk(float a, float b) {
    f32x2 v = {a, b};
    return __builtin_bit_cast(u32, __builtin_convertvector(v, bf16x2_t));
}
__device__ __forceinline__ float fadd(float a, float b) { float d; asm("v_add_f32 %0, %1, %2" : "=v"(d) : "v"(a), "v"(b)); return d; }
__device__ __forceinline__ float ffma(float a, float b, float c) { float d; asm("v_fma_f32 %0, %1, %2, %3" : "=v"(d) : "v"(a), "v"(b), "v"(c)); return d; }
__device__ __forceinline__ bf16x8 ldsr128(u32 a) { return *(const LAS bf16x8*)a; }
__device__ __forceinline__ s16x4 ldstr(u32 a) { return __builtin_amdgcn_ds_read_tr16_b64_v4i16((LAS s16x4*)a); }
__device__ __forceinline__ bf16x8 cat(s16x4 a, s16x4 b) { return __builtin_shufflevector(a, b, 0, 1, 2, 3, 4, 5, 6, 7); }
#define MFMA(a, b, c) __builtin_amdgcn_mfma_f32_16x16x32_bf16(a, b, c, 0, 0, 0)

__shared__ __attribute__((aligned(1024))) char smem[LDS_BYTES];

struct Stamps { u32 t[32]; };
#define STAMP(k) do { u64 t_; asm volatile("s_memrealtime %0\n\ts_waitcnt lgkmcnt(0)" : "=s"(t_) :: "memory"); stp.t[k] = (u32)t_ | 1u; } while (0)
#define PROF_ROLE(b) (((b) >> 3) < 20 ? 0 : ((b) >> 3) < 30 ? 1 : 2)
#define PROF_NB 256

__device__ __forceinline__ u64 ld_epoch(const Params& p) {
    return __hip_atomic_load((const u64*)((const char*)p.ws + EP_OFF), __ATOMIC_RELAXED, __HIP_MEMORY_SCOPE_AGENT);
}
__device__ __forceinline__ void put_ctl(u64 e) {
    const u32 base = (u32)(uintptr_t)smem;
    *(LAS u64*)(base + CTL_OFF) = e + TAGC;
    *(LAS u32*)(base + CTL_OFF + 8) = 0;
    *(LAS u32x2*)(base + CTL_OFF + 16) = u32x2{0, 0};
}
__device__ __forceinline__ u64 get_tag() { return *(const LAS u64*)((u32)(uintptr_t)smem + CTL_OFF); }
__device__ __forceinline__ void publish(const Params& p, int lane, int fidx, u64 tag) {
    waitv<0>();
    if (lane == 0) {
        const u32 old = __hip_atomic_fetch_add((LAS u32*)((u32)(uintptr_t)smem + CTL_OFF + 8), 1u, __ATOMIC_RELAXED, __HIP_MEMORY_SCOPE_WORKGROUP);
        if (old == 3) __hip_atomic_store((u64*)((char*)p.ws + FL_OFF) + fidx, tag, __ATOMIC_RELAXED, __HIP_MEMORY_SCOPE_AGENT);
    }
}
__device__ __forceinline__ void poll(const u64* f, bool active, u64 tag) {
    for (u32 spins = 0; spins < (1u << 16); ++spins) {
        u64 v = tag;
        if (active) v = __hip_atomic_load(f, __ATOMIC_RELAXED, __HIP_MEMORY_SCOPE_AGENT);
        if (__all(v == tag)) return;
        __builtin_amdgcn_s_sleep(1);
    }
}

__device__ __forceinline__ void wait_ready(bool leader, const u64* f, bool active, u64 tag, u32 word) {
    LAS u32* w = (LAS u32*)((u32)(uintptr_t)smem + CTL_OFF + word);
    if (leader) {
        poll(f, active, tag);
        __hip_atomic_store(w, 1u, __ATOMIC_RELAXED, __HIP_MEMORY_SCOPE_WORKGROUP);
    } else {
        for (u32 spins = 0; spins < (1u << 22); ++spins) {
            if (__builtin_amdgcn_readfirstlane(__hip_atomic_load(w, __ATOMIC_RELAXED, __HIP_MEMORY_SCOPE_WORKGROUP))) break;
            __builtin_amdgcn_s_sleep(1);
        }
    }
    asm volatile("" ::: "memory");
}

constexpr u32 mbslot(int st) { return st % 4 == 3 ? TOP_OFF : (st % 4) * 24576; }
constexpr u32 maslot(int st) { return st % 3 == 2 ? TOP_OFF + 24576 : 73728 + (st % 3) * 8192; }
constexpr u32 MS_OFF = 90112, MZ_OFF = MS_OFF + 3072;
static_assert(MLA <= 20, "slot reuse");
#define MROT 3
constexpr int MAR = 8;
struct MCtx { u32 voffA, voffB[6], wa[4], wb, sca; rsrc_t rq, rv; int rot; };
__device__ __forceinline__ u32 krot(int st, int rot) {
    if (MROT == 0) return st;
    const int k = st + rot;
    return k >= 12 ? k - 12 : k;
}
#define MORD 0
constexpr int mord(int k) {
    constexpr int o0[10] = {0, ~0, 1, ~1, 2, 3, ~2, 4, ~3, 5};
    constexpr int o1[10] = {~0, 0, ~1, 1, ~2, 2, ~3, 3, 4, 5};
    constexpr int o2[10] = {~0, ~1, ~2, ~3, 0, 1, 2, 3, 4, 5};
    return MORD == 0 ? o0[k] : MORD == 1 ? o1[k] : o2[k];
}
template <int P> struct MP {
    static constexpr int ST = P / 10, K = P % 10;
    static constexpr bool isA = mord(K) < 0;
    static constexpr int idx = isA ? ~mord(K) : mord(K);
    static constexpr int ar = (ST * 4 + idx) % MAR;
    static __device__ __forceinline__ void issue(u32x4 (&r)[MAR], const MCtx& c) {
        if constexpr (isA) gld(r[ar], c.voffA, c.rq, krot(ST, c.rot) * 256 + idx * 24576);
        else glds(c.wb + mbslot(ST) + idx * 1024, c.voffB[idx], c.rv, krot(ST, c.rot) * 196608);
    }
    static __device__ __forceinline__ void pro(u32x4 (&r)[MAR], const MCtx& c) {
        issue(r, c);
        if constexpr (P + 1 < MLA) MP<P + 1>::pro(r, c);
    }
    static __device__ __forceinline__ void run(u32x4 (&r)[MAR], const MCtx& c, f32x4& s4, f32x4& z4, Stamps& stp) {
        constexpr int REM = (119 - P) < (MLA - 1) ? (119 - P) : (MLA - 1);
        if constexpr (K == 0) {
            s4 = *(const LAS f32x4*)(c.sca + MS_OFF + krot(ST, c.rot) * 256);
            z4 = *(const LAS f32x4*)(c.sca + MZ_OFF + krot(ST, c.rot) * 256);
        }
        if constexpr (isA) {
            waitv1<REM>(r[ar]);
            const u32x4 q = r[ar];
            u32x2 o;
            o[0] = pk(ffma((float)(int)q[0], s4[0], z4[0]), ffma((float)(int)q[1], s4[1], z4[1]));
            o[1] = pk(ffma((float)(int)q[2], s4[2], z4[2]), ffma((float)(int)q[3], s4[3], z4[3]));
            *(LAS u32x2*)(c.wa[idx] + maslot(ST)) = o;
        } else if constexpr (K == 9) {
            waitv<REM>();
        }
        if constexpr (K == 9) { barw(); STAMP(2 + ST); }
        if constexpr (P + MLA < 120) MP<P + MLA>::issue(r, c);
        if constexpr (P + 1 < 120) MP<P + 1>::run(r, c, s4, z4, stp);
    }
};

struct MSet { bf16x8 a[4]; s16x4 bl[3], bh[3]; };
template <bool DOM, bool DOR>
__device__ __forceinline__ void m_phase(f32x4 (&acc)[4][3], const MSet& m, MSet& r, u32 aa, const u32 (&bb)[3], u32 aoff, u32 boff) {
#pragma unroll
    for (int i = 0; i < 12; ++i) {
        if (DOM) acc[i / 3][i % 3] = MFMA(m.a[i / 3], cat(m.bl[i % 3], m.bh[i % 3]), acc[i / 3][i % 3]);
        if (DOR) {
            if (i == 0) r.bl[0] = ldstr(bb[0] + boff);
            if (i == 1) r.bh[0] = ldstr(bb[0] + boff + 1536);
            if (i == 2) r.a[0] = ldsr128(aa + aoff);
            if (i == 3) r.bl[1] = ldstr(bb[1] + boff);
            if (i == 4) r.bh[1] = ldstr(bb[1] + boff + 1536);
            if (i == 5) r.bl[2] = ldstr(bb[2] + boff);
            if (i == 6) r.bh[2] = ldstr(bb[2] + boff + 1536);
            if (i >= 7 && i < 10) r.a[i - 6] = ldsr128(aa + aoff + (i - 6) * 2048);
        }
        SB();
    }
}

__device__ __forceinline__ void m_prod(const Params& p, int pw, int lane, int et, int gt, int half, Stamps& stp) {
    const u32 base = (u32)(uintptr_t)smem;
    const int q = lane >> 4, c = lane & 15;
    const int kb = half * 768;
    MCtx cx;
    cx.rq = mk(p.qw, D_ * F_ * 4); cx.rv = mk(p.V, F_ * F_ * 2);
    cx.voffA = ((et * 64 + 16 * pw + q) * F_ + kb + 4 * c) * 4;
#pragma unroll
    for (int j = 0; j < 6; ++j) {
        const int P = (6 * pw + j) * 1024 + 16 * lane;
        const int r = P / 384, o = P - r * 384;
        const int sw = ((r >> 1) & 1) | (((r >> 3) & 1) << 1);
        cx.voffB[j] = (kb + r) * (F_ * 2) + gt * 384 + (((o >> 5) ^ sw) << 5) + (o & 16);
    }
#pragma unroll
    for (int i = 0; i < 4; ++i) {
        const int row = 16 * pw + 4 * i + q;
        cx.wa[i] = base + row * 128 + (((c >> 1) ^ ((row >> 1) & 7)) << 4) + (c & 1) * 8;
    }
    cx.wb = base + pw * 6144;
    cx.sca = base + c * 16;
    cx.rot = MROT == 1 ? et % 5 : MROT == 2 ? 2 * (et % 5) : MROT == 3 ? ((et % 5) + 3 * (gt & 3)) % 12 : MROT == 4 ? (gt & 3) : 0;
    u32x4 r[MAR];
    MP<0>::pro(r, cx);
    STAMP(1);
    barn();
    f32x4 s4, z4;
    MP<0>::run(r, cx, s4, z4, stp);
}

__device__ __forceinline__ void m_cons(const Params& p, int wave, int lane, int et, int gt, int half, Stamps& stp) {
    const u32 base = (u32)(uintptr_t)smem;
    const int q = lane >> 4, c = lane & 15;
    const int kb = half * 768;
    {
        const int t = wave * 64 + lane;
        if (t < 192) {
            const u32x2 sv = *(const u32x2*)((const char*)p.sc + (kb + 4 * t) * 2);
            const u32x2 zv = *(const u32x2*)((const char*)p.ze + (kb + 4 * t) * 2);
            f32x4 s4, z4;
            s4[0] = __uint_as_float(sv[0] << 16); s4[1] = __uint_as_float(sv[0] & 0xffff0000u);
            s4[2] = __uint_as_float(sv[1] << 16); s4[3] = __uint_as_float(sv[1] & 0xffff0000u);
            z4[0] = -__uint_as_float(zv[0] << 16) * s4[0]; z4[1] = -__uint_as_float(zv[0] & 0xffff0000u) * s4[1];
            z4[2] = -__uint_as_float(zv[1] << 16) * s4[2]; z4[3] = -__uint_as_float(zv[1] & 0xffff0000u) * s4[3];
            *(LAS f32x4*)(base + MS_OFF + t * 16) = s4;
            *(LAS f32x4*)(base + MZ_OFF + t * 16) = z4;
        } else if (t == 192) {
            put_ctl(ld_epoch(p));
        }
    }
    barw();
    STAMP(1);
    const int qq = c >> 2, pp = c & 3;
    const int sw = (qq >> 1) | ((q & 1) << 1);
    u32 ab[2], bb[3];
    ab[0] = base + c * 128 + ((q ^ ((c >> 1) & 7)) << 4);
    ab[1] = ab[0] ^ 64;
#pragma unroll
    for (int n = 0; n < 3; ++n) bb[n] = base + (8 * q + qq) * 384 + (((3 * wave + n) ^ sw) << 5) + 8 * pp;
    f32x4 acc[4][3] = {};
    MSet s0, s1;
#pragma unroll
    for (int st = 0; st < 12; ++st) {
        SB(); barn(); SB();
        if (st == 0) m_phase<false, true>(acc, s1, s0, ab[0], bb, maslot(st), mbslot(st));
        else m_phase<true, true>(acc, s1, s0, ab[0], bb, maslot(st), mbslot(st));
        m_phase<true, true>(acc, s0, s1, ab[1], bb, maslot(st), mbslot(st) + 12288);
    }
    m_phase<true, false>(acc, s1, s0, 0, bb, 0, 0);
    const rsrc_t rw = mk(p.ws, WS_BYTES);
#pragma unroll
    for (int ebl = 0; ebl < 2; ++ebl)
#pragma unroll
        for (int n = 0; n < 3; ++n) {
            const int nt = 3 * wave + n;
            const int gb = 2 * gt + nt / 6, np = nt % 6;
            u32x4 v;
            v[0] = pk(acc[2 * ebl][n][0], acc[2 * ebl][n][1]);
            v[1] = pk(acc[2 * ebl][n][2], acc[2 * ebl][n][3]);
            v[2] = pk(acc[2 * ebl + 1][n][0], acc[2 * ebl + 1][n][1]);
            v[3] = pk(acc[2 * ebl + 1][n][2], acc[2 * ebl + 1][n][3]);
            const u32 off = MF_OFF + (((half * 16 + gb) * 20 + 2 * et + ebl) * 6 + np) * 1024;
            __builtin_amdgcn_raw_buffer_store_b128(v, rw, lane * 16, off, 16);
        }
}

constexpr u32 xslot(int st) { return st % 4 == 1 ? TOP_OFF : st % 4 == 0 ? 0 : (st % 4 - 1) * 32768; }
#define XROT 0
struct XCtx { u32 vU[2], vX[2], ldsb; rsrc_t ru, rx; int rot; };
template <int ST> struct XP {
    static __device__ __forceinline__ void issue(const XCtx& c) {
        int k = ST;
        if (XROT) { k = ST + c.rot; k = k >= 10 ? k - 10 : k; }
#pragma unroll
        for (int j = 0; j < 4; ++j) {
            glds(c.ldsb + xslot(ST) + 16384 + j * 1024, c.vX[j & 1], c.rx, k * 128 + j * 8 * 1280);
            glds(c.ldsb + xslot(ST) + j * 1024, c.vU[j >> 1], c.ru, k * 64 * 1280 + j * 4 * 1280);
        }
    }
    static __device__ __forceinline__ void pro(const XCtx& c) {
        issue(c);
        if constexpr (ST + 1 < XA) XP<ST + 1>::pro(c);
    }
    static __device__ __forceinline__ void run(const XCtx& c, Stamps& stp) {
        constexpr int REM = (9 - ST) < (XA - 1) ? (9 - ST) : (XA - 1);
        waitv<REM * 8>();
        barn();
        STAMP(2 + ST);
        if constexpr (ST + XA < 10) XP<ST + XA>::issue(c);
        if constexpr (ST + 1 < 10) XP<ST + 1>::run(c, stp);
    }
};
static_assert(XA <= 2, "slot reuse");

struct XSet { s16x4 al[4], ah[4]; bf16x8 b[4]; };
template <bool DOM, bool DOR>
__device__ __forceinline__ void x_phase(f32x4 (&acc)[4][4], const XSet& m, XSet& r, const u32 (&ab)[4], u32 bb, u32 aoff, u32 boff) {
#pragma unroll
    for (int i = 0; i < 16; ++i) {
        if (DOM) acc[i / 4][i % 4] = MFMA(cat(m.al[i / 4], m.ah[i / 4]), m.b[i % 4], acc[i / 4][i % 4]);
        if (DOR) {
            if (i == 0) r.al[0] = ldstr(ab[0] + aoff);
            if (i == 1) r.ah[0] = ldstr(ab[0] + aoff + 1024);
            if (i >= 2 && i < 6) r.b[i - 2] = ldsr128(bb + boff + (i - 2) * 2048);
            if (i >= 6 && i < 12 && !(i & 1)) r.al[(i - 4) / 2] = ldstr(ab[(i - 4) / 2] + aoff);
            if (i >= 6 && i < 12 && (i & 1)) r.ah[(i - 4) / 2] = ldstr(ab[(i - 4) / 2] + aoff + 1024);
        }
        SB();
    }
}

__device__ __forceinline__ void x_prod(const Params& p, int pw, int lane, int sb, int eblk, Stamps& stp) {
    const u32 base = (u32)(uintptr_t)smem;
    const int q = lane >> 4, c = lane & 15;
    XCtx cx;
    cx.ru = mk(p.U, D_ * D_ * 2); cx.rx = mk(p.x, S_ * D_ * 2);
#pragma unroll
    for (int h = 0; h < 2; ++h) {
        cx.vU[h] = (16 * pw + q) * 1280 + eblk * 256 + (((c >> 1) ^ (q | (h << 2))) << 5) + (c & 1) * 16;
        cx.vX[h] = (sb * 128 + 32 * pw + (lane >> 3)) * 1280 + (((lane & 7) ^ ((4 * h + q) & 7)) << 4);
    }
    cx.ldsb = base + pw * 4096;
    cx.rot = XROT == 1 ? (2 * eblk + 5 * (sb & 1)) % 10 : XROT == 2 ? 2 * eblk : (2 * eblk + (sb & 1)) % 10;
    XP<0>::pro(cx);
    STAMP(1);
    XP<0>::run(cx, stp);
}

__device__ __forceinline__ void x_cons(const Params& p, int wave, int lane, int sb, int eblk, Stamps& stp) {
    const u32 base = (u32)(uintptr_t)smem;
    const int q = lane >> 4, c = lane & 15;
    const int we = wave & 1, ws = wave >> 1;
    const int qq = c >> 2, pp = c & 3;
    const int sw = qq | ((q & 1) << 2);
    u64 ep = 0;
    if (wave == 0 && lane == 0) ep = ld_epoch(p);
    u32 ab[4], bb[2];
#pragma unroll
    for (int t = 0; t < 4; ++t) ab[t] = base + (8 * q + qq) * 256 + (((4 * we + t) ^ sw) << 5) + 8 * pp;
    bb[0] = base + 16384 + (64 * ws + c) * 128 + ((q ^ ((c >> 1) & 7)) << 4);
    bb[1] = bb[0] ^ 64;
    f32x4 acc[4][4] = {};
    XSet s0, s1;
#pragma unroll
    for (int st = 0; st < 10; ++st) {
        if (st == 2 && wave == 0 && lane == 0) put_ctl(ep);
        SB(); barn(); SB();
        if (st == 0) x_phase<false, true>(acc, s1, s0, ab, bb[0], xslot(st), xslot(st));
        else x_phase<true, true>(acc, s1, s0, ab, bb[0], xslot(st), xslot(st));
        x_phase<true, true>(acc, s0, s1, ab, bb[1], xslot(st) + 8192, xslot(st));
    }
    x_phase<true, false>(acc, s1, s0, ab, 0, 0, 0);
    const rsrc_t rw = mk(p.ws, WS_BYTES);
#pragma unroll
    for (int ebl = 0; ebl < 2; ++ebl)
#pragma unroll
        for (int u = 0; u < 4; ++u) {
            u32x4 v;
            v[0] = pk(acc[2 * ebl][u][0], acc[2 * ebl][u][1]);
            v[1] = pk(acc[2 * ebl][u][2], acc[2 * ebl][u][3]);
            v[2] = pk(acc[2 * ebl + 1][u][0], acc[2 * ebl + 1][u][1]);
            v[3] = pk(acc[2 * ebl + 1][u][2], acc[2 * ebl + 1][u][3]);
            const u32 off = XF_OFF + ((sb * 20 + 4 * eblk + 2 * we + ebl) * 8 + 4 * ws + u) * 1024;
            __builtin_amdgcn_raw_buffer_store_b128(v, rw, lane * 16, off, 16);
        }
}

constexpr u32 YQ = 1552, YEB = 4 * YQ;
__device__ __forceinline__ u32 add2(u32 a, u32 b) {
    const float lo = fadd(__uint_as_float(a << 16), __uint_as_float(b << 16));
    const float hi = fadd(__uint_as_float(a & 0xffff0000u), __uint_as_float(b & 0xffff0000u));
    return pk(lo, hi);
}
struct YCtx { u32 voff, wa[3]; rsrc_t rw; };
template <int P> struct YP {
    static constexpr int ST = P / 3, J = P % 3;
    static __device__ __forceinline__ void issue(u32x4 (&r)[YLA][2], const YCtx& c) {
        gld_ho(r[P % YLA][0], c.voff, c.rw, (ST * 12 + J) * 1024);
        gld_ho(r[P % YLA][1], c.voff, c.rw, (ST * 12 + J) * 1024 + 16 * 120 * 1024);
    }
    static __device__ __forceinline__ void pro(u32x4 (&r)[YLA][2], const YCtx& c) {
        issue(r, c);
        if constexpr (P + 1 < YLA) YP<P + 1>::pro(r, c);
    }
    static __device__ __forceinline__ void run(u32x4 (&r)[YLA][2], const YCtx& c, Stamps& stp) {
        constexpr int REM = (29 - P) < (YLA - 1) ? (29 - P) : (YLA - 1);
        waitv2<REM * 2>(r[P % YLA][0], r[P % YLA][1]);
        u32x4 o;
#pragma unroll
        for (int e = 0; e < 4; ++e) o[e] = add2(r[P % YLA][0][e], r[P % YLA][1][e]);
        *(LAS u32x4*)(c.wa[J] + ST * 2 * YEB) = o;
        if constexpr (P + YLA < 30) YP<P + YLA>::issue(r, c);
        if constexpr (J == 2) { barw(); STAMP(20 + ST); }
        if constexpr (P + 1 < 30) YP<P + 1>::run(r, c, stp);
    }
};

__device__ __forceinline__ void y_load(const Params& p, int pw, int lane, int gb, u64 tag, bool updater, Stamps& stp) {
    const u32 base = (u32)(uintptr_t)smem;
    const int q = lane >> 4, c = lane & 15;
    YCtx cx;
    cx.rw = mk(p.ws, WS_BYTES);
    cx.voff = MF_OFF + (gb * 120 + 3 * pw) * 1024 + lane * 16;
#pragma unroll
    for (int j = 0; j < 3; ++j) {
        const int ci = 3 * pw + j;
        cx.wa[j] = base + (ci / 6) * YEB + q * YQ + (16 * (ci % 6) + c) * 16;
    }
    const u64* fl = (const u64*)((const char*)p.ws + FL_OFF);
    wait_ready(pw == 0, fl + (gb >> 1) * 20 + lane, lane < 20, tag, 16);
    STAMP(19);
    u32x4 r[YLA][2];
    YP<0>::pro(r, cx);
    YP<0>::run(r, cx, stp);
    if (updater && pw == 0) {
#pragma unroll 1
        for (u32 spins = 0; spins < (1u << 16); ++spins) {
            u32x4 f0, f1;
            gld_ho(f0, FL_OFF + lane * 32, cx.rw, 0);
            gld_ho(f1, FL_OFF + lane * 32 + 16, cx.rw, 0);
            waitv2<0>(f0, f1);
            const u32 tl = (u32)tag, th = (u32)(tag >> 32);
            const bool ok = f0[0] == tl && f0[1] == th && f0[2] == tl && f0[3] == th && f1[0] == tl && f1[1] == th && f1[2] == tl && f1[3] == th;
            if (__all(ok)) break;
            __builtin_amdgcn_s_sleep(1);
        }
        if (lane == 0) __hip_atomic_store((u64*)((char*)p.ws + EP_OFF), tag, __ATOMIC_RELAXED, __HIP_MEMORY_SCOPE_AGENT);
    }
}

__device__ __forceinline__ void y_mma(const Params& p, int w, int lane, int sb, int gb, u64 tag, Stamps& stp) {
    const u32 base = (u32)(uintptr_t)smem;
    const int q = lane >> 4, c = lane & 15;
    const rsrc_t rw = mk(p.ws, WS_BYTES);
    const u64* fl = (const u64*)((const char*)p.ws + FL_OFF);
    wait_ready(w == 0, fl + 160 + sb * 5 + lane, lane < 5, tag, 20);
    STAMP(18);
    bf16x8 xa[20][2];
#pragma unroll
    for (int eb = 0; eb < 20; ++eb) {
#pragma unroll
        for (int u = 0; u < 2; ++u)
            xa[eb][u] = __builtin_bit_cast(bf16x8, __builtin_amdgcn_raw_buffer_load_b128(rw, XF_OFF + (sb * 160 + 2 * w) * 1024 + lane * 16, (eb * 8 + u) * 1024, 16));
        if (2 * eb + 2 > XTH) waitv<XTH>();
    }
    STAMP(19);
    const u32 bb = base + q * YQ + c * 96;
    f32x4 acc[2][6] = {};
    bf16x8 b[YR];
    SB(); barn(); SB();
#pragma unroll
    for (int f = 0; f < YR; ++f) b[f] = ldsr128(bb + (f / 6) * YEB + (f % 6) * 16);
    SB();
#pragma unroll
    for (int f = 0; f < 120; ++f) {
        const int eb = f / 6, n = f % 6;
        acc[0][n] = MFMA(xa[eb][0], b[f % YR], acc[0][n]);
        acc[1][n] = MFMA(xa[eb][1], b[f % YR], acc[1][n]);
        SB();
        if (f + YR < 120) {
            if ((f + YR) % 12 == 0) { barn(); SB(); }
            b[f % YR] = ldsr128(bb + ((f + YR) / 6) * YEB + ((f + YR) % 6) * 16);
            SB();
        }
    }
    STAMP(30);
    const rsrc_t ry = mk(p.y, S_ * F_ * 2);
    const u32 yo = ((sb * 128 + 32 * w + 4 * q) * F_ + gb * 96 + 6 * c) * 2;
#pragma unroll
    for (int u = 0; u < 2; ++u)
#pragma unroll
        for (int r = 0; r < 4; ++r) {
            u32x3 v;
            v[0] = pk(acc[u][0][r], acc[u][1][r]);
            v[1] = pk(acc[u][2][r], acc[u][3][r]);
            v[2] = pk(acc[u][4][r], acc[u][5][r]);
            __builtin_amdgcn_raw_buffer_store_b96(v, ry, yo, (16 * u + r) * F_ * 2, 2);
        }
    asm volatile(".Lqk_end:");
}

__global__ __launch_bounds__(512, 2) void quip_kernel(Params p) {
    const int b = blockIdx.x, xcd = b & 7, i = b >> 3;
    const int lane = threadIdx.x & 63;
    const int wave = __builtin_amdgcn_readfirstlane(threadIdx.x >> 6);
    Stamps stp;
    u32 cpf = 0;
    if (wave == CPFW) {
        u64 pc; u32 len;
        asm volatile("s_getpc_b64 %0\n.Lqk_start:\n\ts_mov_b32 %1, .Lqk_end-.Lqk_start" : "=s"(pc), "=s"(len));
        const rsrc_t rc = mk((const void*)pc, len);
        asm volatile("buffer_load_dword %0, %1, %2, 0 offen" : "=v"(cpf) : "v"(((i % CPFN) * 64 + lane) * 128), "s"(rc) : "memory");
    }
#pragma unroll
    for (int k = 0; k < 32; ++k) stp.t[k] = 0;
    STAMP(0);
    if (wave == 0 && lane == 0) __hip_atomic_store((u32*)((char*)p.ws + (16u << 20)) + b, stp.t[0], __ATOMIC_RELAXED, __HIP_MEMORY_SCOPE_AGENT);
    const int sb2 = 8 * (xcd & 1) + (i & 7), gb2 = 4 * (xcd >> 1) + (i >> 3);
    const int half = xcd & 1, et = 5 * ((xcd >> 1) & 1) + i % 5, gt = 4 * (xcd >> 2) + i / 5;
    const int j = i - 20, sb = 2 * xcd + j / 5, eblk = j % 5;
    if (wave >= 4) {
        if (i < 20) m_prod(p, wave - 4, lane, et, gt, half, stp);
        else if (i < 30) x_prod(p, wave - 4, lane, sb, eblk, stp);
        else barn();
        STAMP(15);
        y_mma(p, wave - 4, lane, sb2, gb2, get_tag(), stp);
    } else {
        int fidx;
        if (i < 20) { m_cons(p, wave, lane, et, gt, half, stp); fidx = gt * 20 + et * 2 + half; }
        else if (i < 30) { x_cons(p, wave, lane, sb, eblk, stp); fidx = 160 + sb * 5 + eblk; }
        else {
            if (wave == 0 && lane == 0) put_ctl(ld_epoch(p));
            barw();
            fidx = b;
        }
        STAMP(15);
        const u64 tag = get_tag();
        publish(p, lane, fidx, tag);
        asm volatile("" :: "v"(cpf));
        STAMP(17);
        y_load(p, wave, lane, gb2, tag, b == 255, stp);
    }
    waitv<0>();
    STAMP(31);
    {
        u32 d = 0;
        if (AMP_SEL >= 100) {
            if (b == 255 && wave == 0) {
                const rsrc_t rw = mk(p.ws, 32u << 20);
                u32x4 v; gld_ho(v, (16u << 20) + lane * 16, rw, 0); waitv1<0>(v);
                const u32 t0 = stp.t[0];
                int mn = min(min((int)(v[0] - t0), (int)(v[1] - t0)), min((int)(v[2] - t0), (int)(v[3] - t0)));
                int mx = max(max((int)(v[0] - t0), (int)(v[1] - t0)), max((int)(v[2] - t0), (int)(v[3] - t0)));
                for (int o = 32; o; o >>= 1) { mn = min(mn, __shfl_xor(mn, o)); mx = max(mx, __shfl_xor(mx, o)); }
                mn = __builtin_amdgcn_readfirstlane(mn); mx = __builtin_amdgcn_readfirstlane(mx);
                d = AMP_SEL == 100 ? (u32)((int)(stp.t[31] - t0) - mn) : (u32)(mx - mn);
            }
        } else if (AMP_SEL >= 0) {
            if (b == AMP_B && wave == AMP_W) d = stp.t[AMP_SEL < 0 ? 0 : AMP_SEL % 100] - stp.t[0];
        }
        if (d) {
            if (d > 5000) d = 5000;
            const u64 ts = __builtin_amdgcn_s_memrealtime();
            while (__builtin_amdgcn_s_memrealtime() - ts < (u64)d * 10) __builtin_amdgcn_s_sleep(8);
        }
    }
}

extern "C" void kernel_launch(void* const* d_in, const int* in_sizes, int n_in, void* d_out, int out_size, void* d_ws,
                              size_t ws_size, hipStream_t stream) {
    Params p;
    p.x = d_in[0]; p.qw = d_in[1]; p.sc = d_in[2]; p.ze = d_in[3]; p.U = d_in[4]; p.V = d_in[5];
    p.y = d_out; p.ws = d_ws;
    quip_kernel<<<256, 512, 0, stream>>>(p);
}
```

```cpp
#define PROF 1
#define AMP_SEL 103
#define AMP_B 0
#define AMP_W 4
#define AMP_BASE 2600
#define AMP_K 0
#include <hip/hip_runtime.h>
#include <stdint.h>
#pragma clang diagnostic ignored "-Wint-to-pointer-cast"

#define MLA 20
#define XA 2
#define YLA 8
#define YR 6
#define XTH 8

#define CPFW 3
#define CPFN 7
typedef unsigned u32;
typedef unsigned long long u64;
using bf16x8 = __attribute__((ext_vector_type(8))) short;
using s16x4 = __attribute__((ext_vector_type(4))) short;
using f32x4 = __attribute__((ext_vector_type(4))) float;
using u32x4 = __attribute__((ext_vector_type(4))) unsigned;
using u32x3 = __attribute__((ext_vector_type(3))) unsigned;
using u32x2 = __attribute__((ext_vector_type(2))) unsigned;
using bf16x2_t = __attribute__((ext_vector_type(2))) __bf16;
using f32x2 = __attribute__((ext_vector_type(2))) float;
#define LAS __attribute__((address_space(3)))
typedef __amdgpu_buffer_rsrc_t rsrc_t;

constexpr int S_ = 2048, D_ = 640, F_ = 1536;
constexpr u32 XF_OFF = 0, XF_BYTES = S_ * D_ * 2;
constexpr u32 MF_OFF = XF_BYTES, MF_BYTES = 2 * D_ * F_ * 2;
constexpr u32 FL_OFF = MF_OFF + MF_BYTES;
constexpr u32 EP_OFF = FL_OFF + 4096;
constexpr u32 WS_BYTES = EP_OFF + 256;
constexpr int LDS_BYTES = 163840;
constexpr u32 CTL_OFF = 162816;
constexpr u32 TOP_OFF = 129024;
constexpr u64 TAGC = 0x9E3779B97F4A7C15ull;

struct Params {
    const void *x, *qw, *sc, *ze, *U, *V;
    void *y, *ws;
};

__device__ __forceinline__ rsrc_t mk(const void* p, u32 bytes) { return __builtin_amdgcn_make_buffer_rsrc((void*)p, (short)0, (int)bytes, 0x00020000); }
__device__ __forceinline__ void glds(u32 m0v, u32 voff, rsrc_t r, u32 soff) {
    asm volatile("s_mov_b32 m0, %0\n\ts_nop 0\n\tbuffer_load_dwordx4 %1, %2, %3 offen lds" ::"s"(m0v), "v"(voff), "s"(r), "s"(soff) : "memory");
}
__device__ __forceinline__ void gld(u32x4& d, u32 voff, rsrc_t r, u32 soff) {
    asm volatile("buffer_load_dwordx4 %0, %1, %2, %3 offen" : "=v"(d) : "v"(voff), "s"(r), "s"(soff) : "memory");
}
__device__ __forceinline__ void gld_ho(u32x4& d, u32 voff, rsrc_t r, u32 soff) {
    asm volatile("buffer_load_dwordx4 %0, %1, %2, %3 offen sc1" : "=v"(d) : "v"(voff), "s"(r), "s"(soff) : "memory");
}
__device__ __forceinline__ void gld_ho2(u32x2& d, u32 voff, rsrc_t r, u32 soff) {
    asm volatile("buffer_load_dwordx2 %0, %1, %2, %3 offen sc1" : "=v"(d) : "v"(voff), "s"(r), "s"(soff) : "memory");
}
template <int N> __device__ __forceinline__ void waitv() { asm volatile("s_waitcnt vmcnt(%0)" ::"n"(N) : "memory"); }
template <int N, class T> __device__ __forceinline__ void waitv1(T& a) { asm volatile("s_waitcnt vmcnt(%1)" : "+v"(a) : "n"(N) : "memory"); }
template <int N, class T> __device__ __forceinline__ void waitv2(T& a, T& b) { asm volatile("s_waitcnt vmcnt(%2)" : "+v"(a), "+v"(b) : "n"(N) : "memory"); }
__device__ __forceinline__ void barw() { asm volatile("s_waitcnt lgkmcnt(0)\n\ts_barrier" ::: "memory"); }
__device__ __forceinline__ void barn() { asm volatile("s_barrier" ::: "memory"); }
#define SB() __builtin_amdgcn_sched_barrier(0)
__device__ __forceinline__ u32 pk(float a, float b) {
    f32x2 v = {a, b};
    return __builtin_bit_cast(u32, __builtin_convertvector(v, bf16x2_t));
}
__device__ __forceinline__ float fadd(float a, float b) { float d; asm("v_add_f32 %0, %1, %2" : "=v"(d) : "v"(a), "v"(b)); return d; }
__device__ __forceinline__ float ffma(float a, float b, float c) { float d; asm("v_fma_f32 %0, %1, %2, %3" : "=v"(d) : "v"(a), "v"(b), "v"(c)); return d; }
__device__ __forceinline__ bf16x8 ldsr128(u32 a) { return *(const LAS bf16x8*)a; }
__device__ __forceinline__ s16x4 ldstr(u32 a) { return __builtin_amdgcn_ds_read_tr16_b64_v4i16((LAS s16x4*)a); }
__device__ __forceinline__ bf16x8 cat(s16x4 a, s16x4 b) { return __builtin_shufflevector(a, b, 0, 1, 2, 3, 4, 5, 6, 7); }
#define MFMA(a, b, c) __builtin_amdgcn_mfma_f32_16x16x32_bf16(a, b, c, 0, 0, 0)

__shared__ __attribute__((aligned(1024))) char smem[LDS_BYTES];

struct Stamps { u32 t[32]; };
#define STAMP(k) do { u64 t_; asm volatile("s_memrealtime %0\n\ts_waitcnt lgkmcnt(0)" : "=s"(t_) :: "memory"); stp.t[k] = (u32)t_ | 1u; } while (0)
#define PROF_ROLE(b) (((b) >> 3) < 20 ? 0 : ((b) >> 3) < 30 ? 1 : 2)
#define PROF_NB 256

__device__ __forceinline__ u64 ld_epoch(const Params& p) {
    return __hip_atomic_load((const u64*)((const char*)p.ws + EP_OFF), __ATOMIC_RELAXED, __HIP_MEMORY_SCOPE_AGENT);
}
__device__ __forceinline__ void put_ctl(u64 e) {
    const u32 base = (u32)(uintptr_t)smem;
    *(LAS u64*)(base + CTL_OFF) = e + TAGC;
    *(LAS u32*)(base + CTL_OFF + 8) = 0;
    *(LAS u32x2*)(base + CTL_OFF + 16) = u32x2{0, 0};
}
__device__ __forceinline__ u64 get_tag() { return *(const LAS u64*)((u32)(uintptr_t)smem + CTL_OFF); }
__device__ __forceinline__ void publish(const Params& p, int lane, int fidx, u64 tag) {
    waitv<0>();
    if (lane == 0) {
        const u32 old = __hip_atomic_fetch_add((LAS u32*)((u32)(uintptr_t)smem + CTL_OFF + 8), 1u, __ATOMIC_RELAXED, __HIP_MEMORY_SCOPE_WORKGROUP);
        if (old == 3) __hip_atomic_store((u64*)((char*)p.ws + FL_OFF) + fidx, tag, __ATOMIC_RELAXED, __HIP_MEMORY_SCOPE_AGENT);
    }
}
__device__ __forceinline__ void poll(const u64* f, bool active, u64 tag) {
    for (u32 spins = 0; spins < (1u << 16); ++spins) {
        u64 v = tag;
        if (active) v = __hip_atomic_load(f, __ATOMIC_RELAXED, __HIP_MEMORY_SCOPE_AGENT);
        if (__all(v == tag)) return;
        __builtin_amdgcn_s_sleep(1);
    }
}

__device__ __forceinline__ void wait_ready(bool leader, const u64* f, bool active, u64 tag, u32 word) {
    LAS u32* w = (LAS u32*)((u32)(uintptr_t)smem + CTL_OFF + word);
    if (leader) {
        poll(f, active, tag);
        __hip_atomic_store(w, 1u, __ATOMIC_RELAXED, __HIP_MEMORY_SCOPE_WORKGROUP);
    } else {
        for (u32 spins = 0; spins < (1u << 22); ++spins) {
            if (__builtin_amdgcn_readfirstlane(__hip_atomic_load(w, __ATOMIC_RELAXED, __HIP_MEMORY_SCOPE_WORKGROUP))) break;
            __builtin_amdgcn_s_sleep(1);
        }
    }
    asm volatile("" ::: "memory");
}

constexpr u32 mbslot(int st) { return st % 4 == 3 ? TOP_OFF : (st % 4) * 24576; }
constexpr u32 maslot(int st) { return st % 3 == 2 ? TOP_OFF + 24576 : 73728 + (st % 3) * 8192; }
constexpr u32 MS_OFF = 90112, MZ_OFF = MS_OFF + 3072;
static_assert(MLA <= 20, "slot reuse");
#define MROT 3
constexpr int MAR = 8;
struct MCtx { u32 voffA, voffB[6], wa[4], wb, sca; rsrc_t rq, rv; int rot; };
__device__ __forceinline__ u32 krot(int st, int rot) {
    if (MROT == 0) return st;
    const int k = st + rot;
    return k >= 12 ? k - 12 : k;
}
#define MORD 0
constexpr int mord(int k) {
    constexpr int o0[10] = {0, ~0, 1, ~1, 2, 3, ~2, 4, ~3, 5};
    constexpr int o1[10] = {~0, 0, ~1, 1, ~2, 2, ~3, 3, 4, 5};
    constexpr int o2[10] = {~0, ~1, ~2, ~3, 0, 1, 2, 3, 4, 5};
    return MORD == 0 ? o0[k] : MORD == 1 ? o1[k] : o2[k];
}
template <int P> struct MP {
    static constexpr int ST = P / 10, K = P % 10;
    static constexpr bool isA = mord(K) < 0;
    static constexpr int idx = isA ? ~mord(K) : mord(K);
    static constexpr int ar = (ST * 4 + idx) % MAR;
    static __device__ __forceinline__ void issue(u32x4 (&r)[MAR], const MCtx& c) {
        if constexpr (isA) gld(r[ar], c.voffA, c.rq, krot(ST, c.rot) * 256 + idx * 24576);
        else glds(c.wb + mbslot(ST) + idx * 1024, c.voffB[idx], c.rv, krot(ST, c.rot) * 196608);
    }
    static __device__ __forceinline__ void pro(u32x4 (&r)[MAR], const MCtx& c) {
        issue(r, c);
        if constexpr (P + 1 < MLA) MP<P + 1>::pro(r, c);
    }
    static __device__ __forceinline__ void run(u32x4 (&r)[MAR], const MCtx& c, f32x4& s4, f32x4& z4, Stamps& stp) {
        constexpr int REM = (119 - P) < (MLA - 1) ? (119 - P) : (MLA - 1);
        if constexpr (K == 0) {
            s4 = *(const LAS f32x4*)(c.sca + MS_OFF + krot(ST, c.rot) * 256);
            z4 = *(const LAS f32x4*)(c.sca + MZ_OFF + krot(ST, c.rot) * 256);
        }
        if constexpr (isA) {
            waitv1<REM>(r[ar]);
            const u32x4 q = r[ar];
            u32x2 o;
            o[0] = pk(ffma((float)(int)q[0], s4[0], z4[0]), ffma((float)(int)q[1], s4[1], z4[1]));
            o[1] = pk(ffma((float)(int)q[2], s4[2], z4[2]), ffma((float)(int)q[3], s4[3], z4[3]));
            *(LAS u32x2*)(c.wa[idx] + maslot(ST)) = o;
        } else if constexpr (K == 9) {
            waitv<REM>();
        }
        if constexpr (K == 9) { barw(); STAMP(2 + ST); }
        if constexpr (P + MLA < 120) MP<P + MLA>::issue(r, c);
        if constexpr (P + 1 < 120) MP<P + 1>::run(r, c, s4, z4, stp);
    }
};

struct MSet { bf16x8 a[4]; s16x4 bl[3], bh[3]; };
template <bool DOM, bool DOR>
__device__ __forceinline__ void m_phase(f32x4 (&acc)[4][3], const MSet& m, MSet& r, u32 aa, const u32 (&bb)[3], u32 aoff, u32 boff) {
#pragma unroll
    for (int i = 0; i < 12; ++i) {
        if (DOM) acc[i / 3][i % 3] = MFMA(m.a[i / 3], cat(m.bl[i % 3], m.bh[i % 3]), acc[i / 3][i % 3]);
        if (DOR) {
            if (i == 0) r.bl[0] = ldstr(bb[0] + boff);
            if (i == 1) r.bh[0] = ldstr(bb[0] + boff + 1536);
            if (i == 2) r.a[0] = ldsr128(aa + aoff);
            if (i == 3) r.bl[1] = ldstr(bb[1] + boff);
            if (i == 4) r.bh[1] = ldstr(bb[1] + boff + 1536);
            if (i == 5) r.bl[2] = ldstr(bb[2] + boff);
            if (i == 6) r.bh[2] = ldstr(bb[2] + boff + 1536);
            if (i >= 7 && i < 10) r.a[i - 6] = ldsr128(aa + aoff + (i - 6) * 2048);
        }
        SB();
    }
}

__device__ __forceinline__ void m_prod(const Params& p, int pw, int lane, int et, int gt, int half, Stamps& stp) {
    const u32 base = (u32)(uintptr_t)smem;
    const int q = lane >> 4, c = lane & 15;
    const int kb = half * 768;
    MCtx cx;
    cx.rq = mk(p.qw, D_ * F_ * 4); cx.rv = mk(p.V, F_ * F_ * 2);
    cx.voffA = ((et * 64 + 16 * pw + q) * F_ + kb + 4 * c) * 4;
#pragma unroll
    for (int j = 0; j < 6; ++j) {
        const int P = (6 * pw + j) * 1024 + 16 * lane;
        const int r = P / 384, o = P - r * 384;
        const int sw = ((r >> 1) & 1) | (((r >> 3) & 1) << 1);
        cx.voffB[j] = (kb + r) * (F_ * 2) + gt * 384 + (((o >> 5) ^ sw) << 5) + (o & 16);
    }
#pragma unroll
    for (int i = 0; i < 4; ++i) {
        const int row = 16 * pw + 4 * i + q;
        cx.wa[i] = base + row * 128 + (((c >> 1) ^ ((row >> 1) & 7)) << 4) + (c & 1) * 8;
    }
    cx.wb = base + pw * 6144;
    cx.sca = base + c * 16;
    cx.rot = MROT == 1 ? et % 5 : MROT == 2 ? 2 * (et % 5) : MROT == 3 ? ((et % 5) + 3 * (gt & 3)) % 12 : MROT == 4 ? (gt & 3) : 0;
    u32x4 r[MAR];
    MP<0>::pro(r, cx);
    STAMP(1);
    barn();
    f32x4 s4, z4;
    MP<0>::run(r, cx, s4, z4, stp);
}

__device__ __forceinline__ void m_cons(const Params& p, int wave, int lane, int et, int gt, int half, Stamps& stp) {
    const u32 base = (u32)(uintptr_t)smem;
    const int q = lane >> 4, c = lane & 15;
    const int kb = half * 768;
    {
        const int t = wave * 64 + lane;
        if (t < 192) {
            const u32x2 sv = *(const u32x2*)((const char*)p.sc + (kb + 4 * t) * 2);
            const u32x2 zv = *(const u32x2*)((const char*)p.ze + (kb + 4 * t) * 2);
            f32x4 s4, z4;
            s4[0] = __uint_as_float(sv[0] << 16); s4[1] = __uint_as_float(sv[0] & 0xffff0000u);
            s4[2] = __uint_as_float(sv[1] << 16); s4[3] = __uint_as_float(sv[1] & 0xffff0000u);
            z4[0] = -__uint_as_float(zv[0] << 16) * s4[0]; z4[1] = -__uint_as_float(zv[0] & 0xffff0000u) * s4[1];
            z4[2] = -__uint_as_float(zv[1] << 16) * s4[2]; z4[3] = -__uint_as_float(zv[1] & 0xffff0000u) * s4[3];
            *(LAS f32x4*)(base + MS_OFF + t * 16) = s4;
            *(LAS f32x4*)(base + MZ_OFF + t * 16) = z4;
        } else if (t == 192) {
            put_ctl(ld_epoch(p));
        }
    }
    barw();
    STAMP(1);
    const int qq = c >> 2, pp = c & 3;
    const int sw = (qq >> 1) | ((q & 1) << 1);
    u32 ab[2], bb[3];
    ab[0] = base + c * 128 + ((q ^ ((c >> 1) & 7)) << 4);
    ab[1] = ab[0] ^ 64;
#pragma unroll
    for (int n = 0; n < 3; ++n) bb[n] = base + (8 * q + qq) * 384 + (((3 * wave + n) ^ sw) << 5) + 8 * pp;
    f32x4 acc[4][3] = {};
    MSet s0, s1;
#pragma unroll
    for (int st = 0; st < 12; ++st) {
        SB(); barn(); SB();
        if (st == 0) m_phase<false, true>(acc, s1, s0, ab[0], bb, maslot(st), mbslot(st));
        else m_phase<true, true>(acc, s1, s0, ab[0], bb, maslot(st), mbslot(st));
        m_phase<true, true>(acc, s0, s1, ab[1], bb, maslot(st), mbslot(st) + 12288);
    }
    m_phase<true, false>(acc, s1, s0, 0, bb, 0, 0);
    const rsrc_t rw = mk(p.ws, WS_BYTES);
#pragma unroll
    for (int ebl = 0; ebl < 2; ++ebl)
#pragma unroll
        for (int n = 0; n < 3; ++n) {
            const int nt = 3 * wave + n;
            const int gb = 2 * gt + nt / 6, np = nt % 6;
            u32x4 v;
            v[0] = pk(acc[2 * ebl][n][0], acc[2 * ebl][n][1]);
            v[1] = pk(acc[2 * ebl][n][2], acc[2 * ebl][n][3]);
            v[2] = pk(acc[2 * ebl + 1][n][0], acc[2 * ebl + 1][n][1]);
            v[3] = pk(acc[2 * ebl + 1][n][2], acc[2 * ebl + 1][n][3]);
            const u32 off = MF_OFF + (((half * 16 + gb) * 20 + 2 * et + ebl) * 6 + np) * 1024;
            __builtin_amdgcn_raw_buffer_store_b128(v, rw, lane * 16, off, 16);
        }
}

constexpr u32 xslot(int st) { return st % 4 == 1 ? TOP_OFF : st % 4 == 0 ? 0 : (st % 4 - 1) * 32768; }
#define XROT 0
struct XCtx { u32 vU[2], vX[2], ldsb; rsrc_t ru, rx; int rot; };
template <int ST> struct XP {
    static __device__ __forceinline__ void issue(const XCtx& c) {
        int k = ST;
        if (XROT) { k = ST + c.rot; k = k >= 10 ? k - 10 : k; }
#pragma unroll
        for (int j = 0; j < 4; ++j) {
            glds(c.ldsb + xslot(ST) + 16384 + j * 1024, c.vX[j & 1], c.rx, k * 128 + j * 8 * 1280);
            glds(c.ldsb + xslot(ST) + j * 1024, c.vU[j >> 1], c.ru, k * 64 * 1280 + j * 4 * 1280);
        }
    }
    static __device__ __forceinline__ void pro(const XCtx& c) {
        issue(c);
        if constexpr (ST + 1 < XA) XP<ST + 1>::pro(c);
    }
    static __device__ __forceinline__ void run(const XCtx& c, Stamps& stp) {
        constexpr int REM = (9 - ST) < (XA - 1) ? (9 - ST) : (XA - 1);
        waitv<REM * 8>();
        barn();
        STAMP(2 + ST);
        if constexpr (ST + XA < 10) XP<ST + XA>::issue(c);
        if constexpr (ST + 1 < 10) XP<ST + 1>::run(c, stp);
    }
};
static_assert(XA <= 2, "slot reuse");

struct XSet { s16x4 al[4], ah[4]; bf16x8 b[4]; };
template <bool DOM, bool DOR>
__device__ __forceinline__ void x_phase(f32x4 (&acc)[4][4], const XSet& m, XSet& r, const u32 (&ab)[4], u32 bb, u32 aoff, u32 boff) {
#pragma unroll
    for (int i = 0; i < 16; ++i) {
        if (DOM) acc[i / 4][i % 4] = MFMA(cat(m.al[i / 4], m.ah[i / 4]), m.b[i % 4], acc[i / 4][i % 4]);
        if (DOR) {
            if (i == 0) r.al[0] = ldstr(ab[0] + aoff);
            if (i == 1) r.ah[0] = ldstr(ab[0] + aoff + 1024);
            if (i >= 2 && i < 6) r.b[i - 2] = ldsr128(bb + boff + (i - 2) * 2048);
            if (i >= 6 && i < 12 && !(i & 1)) r.al[(i - 4) / 2] = ldstr(ab[(i - 4) / 2] + aoff);
            if (i >= 6 && i < 12 && (i & 1)) r.ah[(i - 4) / 2] = ldstr(ab[(i - 4) / 2] + aoff + 1024);
        }
        SB();
    }
}

__device__ __forceinline__ void x_prod(const Params& p, int pw, int lane, int sb, int eblk, Stamps& stp) {
    const u32 base = (u32)(uintptr_t)smem;
    const int q = lane >> 4, c = lane & 15;
    XCtx cx;
    cx.ru = mk(p.U, D_ * D_ * 2); cx.rx = mk(p.x, S_ * D_ * 2);
#pragma unroll
    for (int h = 0; h < 2; ++h) {
        cx.vU[h] = (16 * pw + q) * 1280 + eblk * 256 + (((c >> 1) ^ (q | (h << 2))) << 5) + (c & 1) * 16;
        cx.vX[h] = (sb * 128 + 32 * pw + (lane >> 3)) * 1280 + (((lane & 7) ^ ((4 * h + q) & 7)) << 4);
    }
    cx.ldsb = base + pw * 4096;
    cx.rot = XROT == 1 ? (2 * eblk + 5 * (sb & 1)) % 10 : XROT == 2 ? 2 * eblk : (2 * eblk + (sb & 1)) % 10;
    XP<0>::pro(cx);
    STAMP(1);
    XP<0>::run(cx, stp);
}

__device__ __forceinline__ void x_cons(const Params& p, int wave, int lane, int sb, int eblk, Stamps& stp) {
    const u32 base = (u32)(uintptr_t)smem;
    const int q = lane >> 4, c = lane & 15;
    const int we = wave & 1, ws = wave >> 1;
    const int qq = c >> 2, pp = c & 3;
    const int sw = qq | ((q & 1) << 2);
    u64 ep = 0;
    if (wave == 0 && lane == 0) ep = ld_epoch(p);
    u32 ab[4], bb[2];
#pragma unroll
    for (int t = 0; t < 4; ++t) ab[t] = base + (8 * q + qq) * 256 + (((4 * we + t) ^ sw) << 5) + 8 * pp;
    bb[0] = base + 16384 + (64 * ws + c) * 128 + ((q ^ ((c >> 1) & 7)) << 4);
    bb[1] = bb[0] ^ 64;
    f32x4 acc[4][4] = {};
    XSet s0, s1;
#pragma unroll
    for (int st = 0; st < 10; ++st) {
        if (st == 2 && wave == 0 && lane == 0) put_ctl(ep);
        SB(); barn(); SB();
        if (st == 0) x_phase<false, true>(acc, s1, s0, ab, bb[0], xslot(st), xslot(st));
        else x_phase<true, true>(acc, s1, s0, ab, bb[0], xslot(st), xslot(st));
        x_phase<true, true>(acc, s0, s1, ab, bb[1], xslot(st) + 8192, xslot(st));
    }
    x_phase<true, false>(acc, s1, s0, ab, 0, 0, 0);
    const rsrc_t rw = mk(p.ws, WS_BYTES);
#pragma unroll
    for (int ebl = 0; ebl < 2; ++ebl)
#pragma unroll
        for (int u = 0; u < 4; ++u) {
            u32x4 v;
            v[0] = pk(acc[2 * ebl][u][0], acc[2 * ebl][u][1]);
            v[1] = pk(acc[2 * ebl][u][2], acc[2 * ebl][u][3]);
            v[2] = pk(acc[2 * ebl + 1][u][0], acc[2 * ebl + 1][u][1]);
            v[3] = pk(acc[2 * ebl + 1][u][2], acc[2 * ebl + 1][u][3]);
            const u32 off = XF_OFF + ((sb * 20 + 4 * eblk + 2 * we + ebl) * 8 + 4 * ws + u) * 1024;
            __builtin_amdgcn_raw_buffer_store_b128(v, rw, lane * 16, off, 16);
        }
}

constexpr u32 YQ = 1552, YEB = 4 * YQ;
__device__ __forceinline__ u32 add2(u32 a, u32 b) {
    const float lo = fadd(__uint_as_float(a << 16), __uint_as_float(b << 16));
    const float hi = fadd(__uint_as_float(a & 0xffff0000u), __uint_as_float(b & 0xffff0000u));
    return pk(lo, hi);
}
struct YCtx { u32 voff, wa[3]; rsrc_t rw; };
template <int P> struct YP {
    static constexpr int ST = P / 3, J = P % 3;
    static __device__ __forceinline__ void issue(u32x4 (&r)[YLA][2], const YCtx& c) {
        gld_ho(r[P % YLA][0], c.voff, c.rw, (ST * 12 + J) * 1024);
        gld_ho(r[P % YLA][1], c.voff, c.rw, (ST * 12 + J) * 1024 + 16 * 120 * 1024);
    }
    static __device__ __forceinline__ void pro(u32x4 (&r)[YLA][2], const YCtx& c) {
        issue(r, c);
        if constexpr (P + 1 < YLA) YP<P + 1>::pro(r, c);
    }
    static __device__ __forceinline__ void run(u32x4 (&r)[YLA][2], const YCtx& c, Stamps& stp) {
        constexpr int REM = (29 - P) < (YLA - 1) ? (29 - P) : (YLA - 1);
        waitv2<REM * 2>(r[P % YLA][0], r[P % YLA][1]);
        u32x4 o;
#pragma unroll
        for (int e = 0; e < 4; ++e) o[e] = add2(r[P % YLA][0][e], r[P % YLA][1][e]);
        *(LAS u32x4*)(c.wa[J] + ST * 2 * YEB) = o;
        if constexpr (P + YLA < 30) YP<P + YLA>::issue(r, c);
        if constexpr (J == 2) { barw(); STAMP(20 + ST); }
        if constexpr (P + 1 < 30) YP<P + 1>::run(r, c, stp);
    }
};

__device__ __forceinline__ void y_load(const Params& p, int pw, int lane, int gb, u64 tag, bool updater, Stamps& stp) {
    const u32 base = (u32)(uintptr_t)smem;
    const int q = lane >> 4, c = lane & 15;
    YCtx cx;
    cx.rw = mk(p.ws, WS_BYTES);
    cx.voff = MF_OFF + (gb * 120 + 3 * pw) * 1024 + lane * 16;
#pragma unroll
    for (int j = 0; j < 3; ++j) {
        const int ci = 3 * pw + j;
        cx.wa[j] = base + (ci / 6) * YEB + q * YQ + (16 * (ci % 6) + c) * 16;
    }
    const u64* fl = (const u64*)((const char*)p.ws + FL_OFF);
    wait_ready(pw == 0, fl + (gb >> 1) * 20 + lane, lane < 20, tag, 16);
    STAMP(19);
    u32x4 r[YLA][2];
    YP<0>::pro(r, cx);
    YP<0>::run(r, cx, stp);
    if (updater && pw == 0) {
#pragma unroll 1
        for (u32 spins = 0; spins < (1u << 16); ++spins) {
            u32x4 f0, f1;
            gld_ho(f0, FL_OFF + lane * 32, cx.rw, 0);
            gld_ho(f1, FL_OFF + lane * 32 + 16, cx.rw, 0);
            waitv2<0>(f0, f1);
            const u32 tl = (u32)tag, th = (u32)(tag >> 32);
            const bool ok = f0[0] == tl && f0[1] == th && f0[2] == tl && f0[3] == th && f1[0] == tl && f1[1] == th && f1[2] == tl && f1[3] == th;
            if (__all(ok)) break;
            __builtin_amdgcn_s_sleep(1);
        }
        if (lane == 0) __hip_atomic_store((u64*)((char*)p.ws + EP_OFF), tag, __ATOMIC_RELAXED, __HIP_MEMORY_SCOPE_AGENT);
    }
}

__device__ __forceinline__ void y_mma(const Params& p, int w, int lane, int sb, int gb, u64 tag, Stamps& stp) {
    const u32 base = (u32)(uintptr_t)smem;
    const int q = lane >> 4, c = lane & 15;
    const rsrc_t rw = mk(p.ws, WS_BYTES);
    const u64* fl = (const u64*)((const char*)p.ws + FL_OFF);
    wait_ready(w == 0, fl + 160 + sb * 5 + lane, lane < 5, tag, 20);
    STAMP(18);
    bf16x8 xa[20][2];
#pragma unroll
    for (int eb = 0; eb < 20; ++eb) {
#pragma unroll
        for (int u = 0; u < 2; ++u)
            xa[eb][u] = __builtin_bit_cast(bf16x8, __builtin_amdgcn_raw_buffer_load_b128(rw, XF_OFF + (sb * 160 + 2 * w) * 1024 + lane * 16, (eb * 8 + u) * 1024, 16));
        if (2 * eb + 2 > XTH) waitv<XTH>();
    }
    STAMP(19);
    const u32 bb = base + q * YQ + c * 96;
    f32x4 acc[2][6] = {};
    bf16x8 b[YR];
    SB(); barn(); SB();
#pragma unroll
    for (int f = 0; f < YR; ++f) b[f] = ldsr128(bb + (f / 6) * YEB + (f % 6) * 16);
    SB();
#pragma unroll
    for (int f = 0; f < 120; ++f) {
        const int eb = f / 6, n = f % 6;
        acc[0][n] = MFMA(xa[eb][0], b[f % YR], acc[0][n]);
        acc[1][n] = MFMA(xa[eb][1], b[f % YR], acc[1][n]);
        SB();
        if (f + YR < 120) {
            if ((f + YR) % 12 == 0) { barn(); SB(); }
            b[f % YR] = ldsr128(bb + ((f + YR) / 6) * YEB + ((f + YR) % 6) * 16);
            SB();
        }
    }
    STAMP(30);
    const rsrc_t ry = mk(p.y, S_ * F_ * 2);
    const u32 yo = ((sb * 128 + 32 * w + 4 * q) * F_ + gb * 96 + 6 * c) * 2;
#pragma unroll
    for (int u = 0; u < 2; ++u)
#pragma unroll
        for (int r = 0; r < 4; ++r) {
            u32x3 v;
            v[0] = pk(acc[u][0][r], acc[u][1][r]);
            v[1] = pk(acc[u][2][r], acc[u][3][r]);
            v[2] = pk(acc[u][4][r], acc[u][5][r]);
            __builtin_amdgcn_raw_buffer_store_b96(v, ry, yo, (16 * u + r) * F_ * 2, 2);
        }
    asm volatile(".Lqk_end:");
}

__global__ __launch_bounds__(512, 2) void quip_kernel(Params p) {
    const int b = blockIdx.x, xcd = b & 7, i = b >> 3;
    const int lane = threadIdx.x & 63;
    const int wave = __builtin_amdgcn_readfirstlane(threadIdx.x >> 6);
    Stamps stp;
    u32 cpf = 0;
    if (wave == CPFW) {
        u64 pc; u32 len;
        asm volatile("s_getpc_b64 %0\n.Lqk_start:\n\ts_mov_b32 %1, .Lqk_end-.Lqk_start" : "=s"(pc), "=s"(len));
        const rsrc_t rc = mk((const void*)pc, len);
        asm volatile("buffer_load_dword %0, %1, %2, 0 offen" : "=v"(cpf) : "v"(((i % CPFN) * 64 + lane) * 128), "s"(rc) : "memory");
    }
#pragma unroll
    for (int k = 0; k < 32; ++k) stp.t[k] = 0;
    STAMP(0);
    if (wave == 0 && lane == 0) __hip_atomic_store((u32*)((char*)p.ws + (16u << 20)) + b, stp.t[0], __ATOMIC_RELAXED, __HIP_MEMORY_SCOPE_AGENT);
    const int sb2 = 8 * (xcd & 1) + (i & 7), gb2 = 4 * (xcd >> 1) + (i >> 3);
    const int half = xcd & 1, et = 5 * ((xcd >> 1) & 1) + i % 5, gt = 4 * (xcd >> 2) + i / 5;
    const int j = i - 20, sb = 2 * xcd + j / 5, eblk = j % 5;
    if (wave >= 4) {
        if (i < 20) m_prod(p, wave - 4, lane, et, gt, half, stp);
        else if (i < 30) x_prod(p, wave - 4, lane, sb, eblk, stp);
        else barn();
        STAMP(15);
        y_mma(p, wave - 4, lane, sb2, gb2, get_tag(), stp);
    } else {
        int fidx;
        if (i < 20) { m_cons(p, wave, lane, et, gt, half, stp); fidx = gt * 20 + et * 2 + half; }
        else if (i < 30) { x_cons(p, wave, lane, sb, eblk, stp); fidx = 160 + sb * 5 + eblk; }
        else {
            if (wave == 0 && lane == 0) put_ctl(ld_epoch(p));
            barw();
            fidx = b;
        }
        STAMP(15);
        const u64 tag = get_tag();
        publish(p, lane, fidx, tag);
        asm volatile("" :: "v"(cpf));
        STAMP(17);
        y_load(p, wave, lane, gb2, tag, b == 255, stp);
    }
    waitv<0>();
    STAMP(31);
    {
        u32 d = 0;
        const u32 tg = (u32)get_tag();
        if (AMP_SEL == 102 && wave >= 4 && lane == 0)
            __hip_atomic_store((u64*)((char*)p.ws + (16u << 20) + 4096) + b * 4 + wave - 4, ((u64)tg << 32) | stp.t[31], __ATOMIC_RELAXED, __HIP_MEMORY_SCOPE_AGENT);
        if (AMP_SEL >= 100) {
            if (b == 255 && wave == 0) {
                const rsrc_t rw = mk(p.ws, 32u << 20);
                u32x4 v; gld_ho(v, (16u << 20) + lane * 16, rw, 0); waitv1<0>(v);
                const u32 t0 = stp.t[0];
                int mn = min(min((int)(v[0] - t0), (int)(v[1] - t0)), min((int)(v[2] - t0), (int)(v[3] - t0)));
                int mx = max(max((int)(v[0] - t0), (int)(v[1] - t0)), max((int)(v[2] - t0), (int)(v[3] - t0)));
                for (int o = 32; o; o >>= 1) { mn = min(mn, __shfl_xor(mn, o)); mx = max(mx, __shfl_xor(mx, o)); }
                mn = __builtin_amdgcn_readfirstlane(mn); mx = __builtin_amdgcn_readfirstlane(mx);
                d = AMP_SEL == 100 ? (u32)((int)(stp.t[31] - t0) - mn) : AMP_SEL == 101 ? (u32)(mx - mn) : 200;
                if (AMP_SEL == 102) {
                    int me = 0;
                    for (int k = 0; k < 8; ++k) {
                        for (u32 spins = 0; spins < 100000; ++spins) {
                            gld_ho(v, (16u << 20) + 4096 + k * 1024 + lane * 16, rw, 0); waitv1<0>(v);
                            if (__all(v[1] == tg && v[3] == tg)) break;
                        }
                        me = max(me, max((int)(v[0] - t0), (int)(v[2] - t0)));
                    }
                    for (int o = 32; o; o >>= 1) me = max(me, __shfl_xor(me, o));
                    me = __builtin_amdgcn_readfirstlane(me);
                    d = me - mn;
                }
                const u32 tgt = t0 + mn + AMP_BASE + d * AMP_K;
                while ((int)((u32)__builtin_amdgcn_s_memrealtime() - tgt) < 0) __builtin_amdgcn_s_sleep(4);
            }
        } else if (AMP_SEL >= 0) {
            if (b == AMP_B && wave == AMP_W) {
                d = stp.t[AMP_SEL < 0 ? 0 : AMP_SEL % 100] - stp.t[0];
                const u32 tgt = stp.t[0] + AMP_BASE + d * AMP_K;
                while ((int)((u32)__builtin_amdgcn_s_memrealtime() - tgt) < 0) __builtin_amdgcn_s_sleep(4);
            }
        }
    }
}

extern "C" void kernel_launch(void* const* d_in, const int* in_sizes, int n_in, void* d_out, int out_size, void* d_ws,
                              size_t ws_size, hipStream_t stream) {
    Params p;
    p.x = d_in[0]; p.qw = d_in[1]; p.sc = d_in[2]; p.ze = d_in[3]; p.U = d_in[4]; p.V = d_in[5];
    p.y = d_out; p.ws = d_ws;
    quip_kernel<<<256, 512, 0, stream>>>(p);
}
```

```cpp
#define PROF 1
#define AMP_SEL -1
#define AMP_B 0
#define AMP_W 4
#define AMP_BASE 4000
#define AMP_K 0
#define PRE_G 1
#define PRE_B 64
#include <hip/hip_runtime.h>
#include <stdint.h>
#pragma clang diagnostic ignored "-Wint-to-pointer-cast"

#define MLA 20
#define XA 2
#define YLA 8
#define YR 6
#define XTH 8

#define CPFW 3
#define CPFN 7
typedef unsigned u32;
typedef unsigned long long u64;
using bf16x8 = __attribute__((ext_vector_type(8))) short;
using s16x4 = __attribute__((ext_vector_type(4))) short;
using f32x4 = __attribute__((ext_vector_type(4))) float;
using u32x4 = __attribute__((ext_vector_type(4))) unsigned;
using u32x3 = __attribute__((ext_vector_type(3))) unsigned;
using u32x2 = __attribute__((ext_vector_type(2))) unsigned;
using bf16x2_t = __attribute__((ext_vector_type(2))) __bf16;
using f32x2 = __attribute__((ext_vector_type(2))) float;
#define LAS __attribute__((address_space(3)))
typedef __amdgpu_buffer_rsrc_t rsrc_t;

constexpr int S_ = 2048, D_ = 640, F_ = 1536;
constexpr u32 XF_OFF = 0, XF_BYTES = S_ * D_ * 2;
constexpr u32 MF_OFF = XF_BYTES, MF_BYTES = 2 * D_ * F_ * 2;
constexpr u32 FL_OFF = MF_OFF + MF_BYTES;
constexpr u32 EP_OFF = FL_OFF + 4096;
constexpr u32 WS_BYTES = EP_OFF + 256;
constexpr int LDS_BYTES = 163840;
constexpr u32 CTL_OFF = 162816;
constexpr u32 TOP_OFF = 129024;
constexpr u64 TAGC = 0x9E3779B97F4A7C15ull;

struct Params {
    const void *x, *qw, *sc, *ze, *U, *V;
    void *y, *ws;
};

__device__ __forceinline__ rsrc_t mk(const void* p, u32 bytes) { return __builtin_amdgcn_make_buffer_rsrc((void*)p, (short)0, (int)bytes, 0x00020000); }
__device__ __forceinline__ void glds(u32 m0v, u32 voff, rsrc_t r, u32 soff) {
    asm volatile("s_mov_b32 m0, %0\n\ts_nop 0\n\tbuffer_load_dwordx4 %1, %2, %3 offen lds" ::"s"(m0v), "v"(voff), "s"(r), "s"(soff) : "memory");
}
__device__ __forceinline__ void gld(u32x4& d, u32 voff, rsrc_t r, u32 soff) {
    asm volatile("buffer_load_dwordx4 %0, %1, %2, %3 offen" : "=v"(d) : "v"(voff), "s"(r), "s"(soff) : "memory");
}
__device__ __forceinline__ void gld_ho(u32x4& d, u32 voff, rsrc_t r, u32 soff) {
    asm volatile("buffer_load_dwordx4 %0, %1, %2, %3 offen sc1" : "=v"(d) : "v"(voff), "s"(r), "s"(soff) : "memory");
}
__device__ __forceinline__ void gld_ho2(u32x2& d, u32 voff, rsrc_t r, u32 soff) {
    asm volatile("buffer_load_dwordx2 %0, %1, %2, %3 offen sc1" : "=v"(d) : "v"(voff), "s"(r), "s"(soff) : "memory");
}
template <int N> __device__ __forceinline__ void waitv() { asm volatile("s_waitcnt vmcnt(%0)" ::"n"(N) : "memory"); }
template <int N, class T> __device__ __forceinline__ void waitv1(T& a) { asm volatile("s_waitcnt vmcnt(%1)" : "+v"(a) : "n"(N) : "memory"); }
template <int N, class T> __device__ __forceinline__ void waitv2(T& a, T& b) { asm volatile("s_waitcnt vmcnt(%2)" : "+v"(a), "+v"(b) : "n"(N) : "memory"); }
__device__ __forceinline__ void barw() { asm volatile("s_waitcnt lgkmcnt(0)\n\ts_barrier" ::: "memory"); }
__device__ __forceinline__ void barn() { asm volatile("s_barrier" ::: "memory"); }
#define SB() __builtin_amdgcn_sched_barrier(0)
__device__ __forceinline__ u32 pk(float a, float b) {
    f32x2 v = {a, b};
    return __builtin_bit_cast(u32, __builtin_convertvector(v, bf16x2_t));
}
__device__ __forceinline__ float fadd(float a, float b) { float d; asm("v_add_f32 %0, %1, %2" : "=v"(d) : "v"(a), "v"(b)); return d; }
__device__ __forceinline__ float ffma(float a, float b, float c) { float d; asm("v_fma_f32 %0, %1, %2, %3" : "=v"(d) : "v"(a), "v"(b), "v"(c)); return d; }
__device__ __forceinline__ bf16x8 ldsr128(u32 a) { return *(const LAS bf16x8*)a; }
__device__ __forceinline__ s16x4 ldstr(u32 a) { return __builtin_amdgcn_ds_read_tr16_b64_v4i16((LAS s16x4*)a); }
__device__ __forceinline__ bf16x8 cat(s16x4 a, s16x4 b) { return __builtin_shufflevector(a, b, 0, 1, 2, 3, 4, 5, 6, 7); }
#define MFMA(a, b, c) __builtin_amdgcn_mfma_f32_16x16x32_bf16(a, b, c, 0, 0, 0)

__shared__ __attribute__((aligned(1024))) char smem[LDS_BYTES];

struct Stamps { u32 t[32]; };
#define STAMP(k) do { u64 t_; asm volatile("s_memrealtime %0\n\ts_waitcnt lgkmcnt(0)" : "=s"(t_) :: "memory"); stp.t[k] = (u32)t_ | 1u; } while (0)
#define PROF_ROLE(b) (((b) >> 3) < 20 ? 0 : ((b) >> 3) < 30 ? 1 : 2)
#define PROF_NB 256

__device__ __forceinline__ u64 ld_epoch(const Params& p) {
    return __hip_atomic_load((const u64*)((const char*)p.ws + EP_OFF), __ATOMIC_RELAXED, __HIP_MEMORY_SCOPE_AGENT);
}
__device__ __forceinline__ void put_ctl(u64 e) {
    const u32 base = (u32)(uintptr_t)smem;
    *(LAS u64*)(base + CTL_OFF) = e + TAGC;
    *(LAS u32*)(base + CTL_OFF + 8) = 0;
    *(LAS u32x2*)(base + CTL_OFF + 16) = u32x2{0, 0};
}
__device__ __forceinline__ u64 get_tag() { return *(const LAS u64*)((u32)(uintptr_t)smem + CTL_OFF); }
__device__ __forceinline__ void publish(const Params& p, int lane, int fidx, u64 tag) {
    waitv<0>();
    if (lane == 0) {
        const u32 old = __hip_atomic_fetch_add((LAS u32*)((u32)(uintptr_t)smem + CTL_OFF + 8), 1u, __ATOMIC_RELAXED, __HIP_MEMORY_SCOPE_WORKGROUP);
        if (old == 3) __hip_atomic_store((u64*)((char*)p.ws + FL_OFF) + fidx, tag, __ATOMIC_RELAXED, __HIP_MEMORY_SCOPE_AGENT);
    }
}
__device__ __forceinline__ void poll(const u64* f, bool active, u64 tag) {
    for (u32 spins = 0; spins < (1u << 16); ++spins) {
        u64 v = tag;
        if (active) v = __hip_atomic_load(f, __ATOMIC_RELAXED, __HIP_MEMORY_SCOPE_AGENT);
        if (__all(v == tag)) return;
        __builtin_amdgcn_s_sleep(1);
    }
}

__device__ __forceinline__ void wait_ready(bool leader, const u64* f, bool active, u64 tag, u32 word) {
    LAS u32* w = (LAS u32*)((u32)(uintptr_t)smem + CTL_OFF + word);
    if (leader) {
        poll(f, active, tag);
        __hip_atomic_store(w, 1u, __ATOMIC_RELAXED, __HIP_MEMORY_SCOPE_WORKGROUP);
    } else {
        for (u32 spins = 0; spins < (1u << 22); ++spins) {
            if (__builtin_amdgcn_readfirstlane(__hip_atomic_load(w, __ATOMIC_RELAXED, __HIP_MEMORY_SCOPE_WORKGROUP))) break;
            __builtin_amdgcn_s_sleep(1);
        }
    }
    asm volatile("" ::: "memory");
}

constexpr u32 mbslot(int st) { return st % 4 == 3 ? TOP_OFF : (st % 4) * 24576; }
constexpr u32 maslot(int st) { return st % 3 == 2 ? TOP_OFF + 24576 : 73728 + (st % 3) * 8192; }
constexpr u32 MS_OFF = 90112, MZ_OFF = MS_OFF + 3072;
static_assert(MLA <= 20, "slot reuse");
#define MROT 3
constexpr int MAR = 8;
struct MCtx { u32 voffA, voffB[6], wa[4], wb, sca; rsrc_t rq, rv; int rot; };
__device__ __forceinline__ u32 krot(int st, int rot) {
    if (MROT == 0) return st;
    const int k = st + rot;
    return k >= 12 ? k - 12 : k;
}
#define MORD 0
constexpr int mord(int k) {
    constexpr int o0[10] = {0, ~0, 1, ~1, 2, 3, ~2, 4, ~3, 5};
    constexpr int o1[10] = {~0, 0, ~1, 1, ~2, 2, ~3, 3, 4, 5};
    constexpr int o2[10] = {~0, ~1, ~2, ~3, 0, 1, 2, 3, 4, 5};
    return MORD == 0 ? o0[k] : MORD == 1 ? o1[k] : o2[k];
}
template <int P> struct MP {
    static constexpr int ST = P / 10, K = P % 10;
    static constexpr bool isA = mord(K) < 0;
    static constexpr int idx = isA ? ~mord(K) : mord(K);
    static constexpr int ar = (ST * 4 + idx) % MAR;
    static __device__ __forceinline__ void issue(u32x4 (&r)[MAR], const MCtx& c) {
        if constexpr (isA) gld(r[ar], c.voffA, c.rq, krot(ST, c.rot) * 256 + idx * 24576);
        else glds(c.wb + mbslot(ST) + idx * 1024, c.voffB[idx], c.rv, krot(ST, c.rot) * 196608);
    }
    static __device__ __forceinline__ void pro(u32x4 (&r)[MAR], const MCtx& c) {
        issue(r, c);
        if constexpr (P + 1 < MLA) MP<P + 1>::pro(r, c);
    }
    static __device__ __forceinline__ void run(u32x4 (&r)[MAR], const MCtx& c, f32x4& s4, f32x4& z4, Stamps& stp) {
        constexpr int REM = (119 - P) < (MLA - 1) ? (119 - P) : (MLA - 1);
        if constexpr (K == 0) {
            s4 = *(const LAS f32x4*)(c.sca + MS_OFF + krot(ST, c.rot) * 256);
            z4 = *(const LAS f32x4*)(c.sca + MZ_OFF + krot(ST, c.rot) * 256);
        }
        if constexpr (isA) {
            waitv1<REM>(r[ar]);
            const u32x4 q = r[ar];
            u32x2 o;
            o[0] = pk(ffma((float)(int)q[0], s4[0], z4[0]), ffma((float)(int)q[1], s4[1], z4[1]));
            o[1] = pk(ffma((float)(int)q[2], s4[2], z4[2]), ffma((float)(int)q[3], s4[3], z4[3]));
            *(LAS u32x2*)(c.wa[idx] + maslot(ST)) = o;
        } else if constexpr (K == 9) {
            waitv<REM>();
        }
        if constexpr (K == 9) { barw(); STAMP(2 + ST); }
        if constexpr (P + MLA < 120) MP<P + MLA>::issue(r, c);
        if constexpr (P + 1 < 120) MP<P + 1>::run(r, c, s4, z4, stp);
    }
};

struct MSet { bf16x8 a[4]; s16x4 bl[3], bh[3]; };
template <bool DOM, bool DOR>
__device__ __forceinline__ void m_phase(f32x4 (&acc)[4][3], const MSet& m, MSet& r, u32 aa, const u32 (&bb)[3], u32 aoff, u32 boff) {
#pragma unroll
    for (int i = 0; i < 12; ++i) {
        if (DOM) acc[i / 3][i % 3] = MFMA(m.a[i / 3], cat(m.bl[i % 3], m.bh[i % 3]), acc[i / 3][i % 3]);
        if (DOR) {
            if (i == 0) r.bl[0] = ldstr(bb[0] + boff);
            if (i == 1) r.bh[0] = ldstr(bb[0] + boff + 1536);
            if (i == 2) r.a[0] = ldsr128(aa + aoff);
            if (i == 3) r.bl[1] = ldstr(bb[1] + boff);
            if (i == 4) r.bh[1] = ldstr(bb[1] + boff + 1536);
            if (i == 5) r.bl[2] = ldstr(bb[2] + boff);
            if (i == 6) r.bh[2] = ldstr(bb[2] + boff + 1536);
            if (i >= 7 && i < 10) r.a[i - 6] = ldsr128(aa + aoff + (i - 6) * 2048);
        }
        SB();
    }
}

__device__ __forceinline__ void m_prod(const Params& p, int pw, int lane, int et, int gt, int half, Stamps& stp) {
    const u32 base = (u32)(uintptr_t)smem;
    const int q = lane >> 4, c = lane & 15;
    const int kb = half * 768;
    MCtx cx;
    cx.rq = mk(p.qw, D_ * F_ * 4); cx.rv = mk(p.V, F_ * F_ * 2);
    cx.voffA = ((et * 64 + 16 * pw + q) * F_ + kb + 4 * c) * 4;
#pragma unroll
    for (int j = 0; j < 6; ++j) {
        const int P = (6 * pw + j) * 1024 + 16 * lane;
        const int r = P / 384, o = P - r * 384;
        const int sw = ((r >> 1) & 1) | (((r >> 3) & 1) << 1);
        cx.voffB[j] = (kb + r) * (F_ * 2) + gt * 384 + (((o >> 5) ^ sw) << 5) + (o & 16);
    }
#pragma unroll
    for (int i = 0; i < 4; ++i) {
        const int row = 16 * pw + 4 * i + q;
        cx.wa[i] = base + row * 128 + (((c >> 1) ^ ((row >> 1) & 7)) << 4) + (c & 1) * 8;
    }
    cx.wb = base + pw * 6144;
    cx.sca = base + c * 16;
    cx.rot = MROT == 1 ? et % 5 : MROT == 2 ? 2 * (et % 5) : MROT == 3 ? ((et % 5) + 3 * (gt & 3)) % 12 : MROT == 4 ? (gt & 3) : 0;
    u32x4 r[MAR];
    MP<0>::pro(r, cx);
    STAMP(1);
    barn();
    f32x4 s4, z4;
    MP<0>::run(r, cx, s4, z4, stp);
}

__device__ __forceinline__ void m_cons(const Params& p, int wave, int lane, int et, int gt, int half, Stamps& stp) {
    const u32 base = (u32)(uintptr_t)smem;
    const int q = lane >> 4, c = lane & 15;
    const int kb = half * 768;
    {
        const int t = wave * 64 + lane;
        if (t < 192) {
            const u32x2 sv = *(const u32x2*)((const char*)p.sc + (kb + 4 * t) * 2);
            const u32x2 zv = *(const u32x2*)((const char*)p.ze + (kb + 4 * t) * 2);
            f32x4 s4, z4;
            s4[0] = __uint_as_float(sv[0] << 16); s4[1] = __uint_as_float(sv[0] & 0xffff0000u);
            s4[2] = __uint_as_float(sv[1] << 16); s4[3] = __uint_as_float(sv[1] & 0xffff0000u);
            z4[0] = -__uint_as_float(zv[0] << 16) * s4[0]; z4[1] = -__uint_as_float(zv[0] & 0xffff0000u) * s4[1];
            z4[2] = -__uint_as_float(zv[1] << 16) * s4[2]; z4[3] = -__uint_as_float(zv[1] & 0xffff0000u) * s4[3];
            *(LAS f32x4*)(base + MS_OFF + t * 16) = s4;
            *(LAS f32x4*)(base + MZ_OFF + t * 16) = z4;
        } else if (t == 192) {
            put_ctl(ld_epoch(p));
        }
    }
    barw();
    STAMP(1);
    const int qq = c >> 2, pp = c & 3;
    const int sw = (qq >> 1) | ((q & 1) << 1);
    u32 ab[2], bb[3];
    ab[0] = base + c * 128 + ((q ^ ((c >> 1) & 7)) << 4);
    ab[1] = ab[0] ^ 64;
#pragma unroll
    for (int n = 0; n < 3; ++n) bb[n] = base + (8 * q + qq) * 384 + (((3 * wave + n) ^ sw) << 5) + 8 * pp;
    f32x4 acc[4][3] = {};
    MSet s0, s1;
#pragma unroll
    for (int st = 0; st < 12; ++st) {
        SB(); barn(); SB();
        if (st == 0) m_phase<false, true>(acc, s1, s0, ab[0], bb, maslot(st), mbslot(st));
        else m_phase<true, true>(acc, s1, s0, ab[0], bb, maslot(st), mbslot(st));
        m_phase<true, true>(acc, s0, s1, ab[1], bb, maslot(st), mbslot(st) + 12288);
    }
    m_phase<true, false>(acc, s1, s0, 0, bb, 0, 0);
    const rsrc_t rw = mk(p.ws, WS_BYTES);
#pragma unroll
    for (int ebl = 0; ebl < 2; ++ebl)
#pragma unroll
        for (int n = 0; n < 3; ++n) {
            const int nt = 3 * wave + n;
            const int gb = 2 * gt + nt / 6, np = nt % 6;
            u32x4 v;
            v[0] = pk(acc[2 * ebl][n][0], acc[2 * ebl][n][1]);
            v[1] = pk(acc[2 * ebl][n][2], acc[2 * ebl][n][3]);
            v[2] = pk(acc[2 * ebl + 1][n][0], acc[2 * ebl + 1][n][1]);
            v[3] = pk(acc[2 * ebl + 1][n][2], acc[2 * ebl + 1][n][3]);
            const u32 off = MF_OFF + (((half * 16 + gb) * 20 + 2 * et + ebl) * 6 + np) * 1024;
            __builtin_amdgcn_raw_buffer_store_b128(v, rw, lane * 16, off, 16);
        }
}

constexpr u32 xslot(int st) { return st % 4 == 1 ? TOP_OFF : st % 4 == 0 ? 0 : (st % 4 - 1) * 32768; }
#define XROT 0
struct XCtx { u32 vU[2], vX[2], ldsb; rsrc_t ru, rx; int rot; };
template <int ST> struct XP {
    static __device__ __forceinline__ void issue(const XCtx& c) {
        int k = ST;
        if (XROT) { k = ST + c.rot; k = k >= 10 ? k - 10 : k; }
#pragma unroll
        for (int j = 0; j < 4; ++j) {
            glds(c.ldsb + xslot(ST) + 16384 + j * 1024, c.vX[j & 1], c.rx, k * 128 + j * 8 * 1280);
            glds(c.ldsb + xslot(ST) + j * 1024, c.vU[j >> 1], c.ru, k * 64 * 1280 + j * 4 * 1280);
        }
    }
    static __device__ __forceinline__ void pro(const XCtx& c) {
        issue(c);
        if constexpr (ST + 1 < XA) XP<ST + 1>::pro(c);
    }
    static __device__ __forceinline__ void run(const XCtx& c, Stamps& stp) {
        constexpr int REM = (9 - ST) < (XA - 1) ? (9 - ST) : (XA - 1);
        waitv<REM * 8>();
        barn();
        STAMP(2 + ST);
        if constexpr (ST + XA < 10) XP<ST + XA>::issue(c);
        if constexpr (ST + 1 < 10) XP<ST + 1>::run(c, stp);
    }
};
static_assert(XA <= 2, "slot reuse");

struct XSet { s16x4 al[4], ah[4]; bf16x8 b[4]; };
template <bool DOM, bool DOR>
__device__ __forceinline__ void x_phase(f32x4 (&acc)[4][4], const XSet& m, XSet& r, const u32 (&ab)[4], u32 bb, u32 aoff, u32 boff) {
#pragma unroll
    for (int i = 0; i < 16; ++i) {
        if (DOM) acc[i / 4][i % 4] = MFMA(cat(m.al[i / 4], m.ah[i / 4]), m.b[i % 4], acc[i / 4][i % 4]);
        if (DOR) {
            if (i == 0) r.al[0] = ldstr(ab[0] + aoff);
            if (i == 1) r.ah[0] = ldstr(ab[0] + aoff + 1024);
            if (i >= 2 && i < 6) r.b[i - 2] = ldsr128(bb + boff + (i - 2) * 2048);
            if (i >= 6 && i < 12 && !(i & 1)) r.al[(i - 4) / 2] = ldstr(ab[(i - 4) / 2] + aoff);
            if (i >= 6 && i < 12 && (i & 1)) r.ah[(i - 4) / 2] = ldstr(ab[(i - 4) / 2] + aoff + 1024);
        }
        SB();
    }
}

__device__ __forceinline__ void x_prod(const Params& p, int pw, int lane, int sb, int eblk, Stamps& stp) {
    const u32 base = (u32)(uintptr_t)smem;
    const int q = lane >> 4, c = lane & 15;
    XCtx cx;
    cx.ru = mk(p.U, D_ * D_ * 2); cx.rx = mk(p.x, S_ * D_ * 2);
#pragma unroll
    for (int h = 0; h < 2; ++h) {
        cx.vU[h] = (16 * pw + q) * 1280 + eblk * 256 + (((c >> 1) ^ (q | (h << 2))) << 5) + (c & 1) * 16;
        cx.vX[h] = (sb * 128 + 32 * pw + (lane >> 3)) * 1280 + (((lane & 7) ^ ((4 * h + q) & 7)) << 4);
    }
    cx.ldsb = base + pw * 4096;
    cx.rot = XROT == 1 ? (2 * eblk + 5 * (sb & 1)) % 10 : XROT == 2 ? 2 * eblk : (2 * eblk + (sb & 1)) % 10;
    XP<0>::pro(cx);
    STAMP(1);
    XP<0>::run(cx, stp);
}

__device__ __forceinline__ void x_cons(const Params& p, int wave, int lane, int sb, int eblk, Stamps& stp) {
    const u32 base = (u32)(uintptr_t)smem;
    const int q = lane >> 4, c = lane & 15;
    const int we = wave & 1, ws = wave >> 1;
    const int qq = c >> 2, pp = c & 3;
    const int sw = qq | ((q & 1) << 2);
    u64 ep = 0;
    if (wave == 0 && lane == 0) ep = ld_epoch(p);
    u32 ab[4], bb[2];
#pragma unroll
    for (int t = 0; t < 4; ++t) ab[t] = base + (8 * q + qq) * 256 + (((4 * we + t) ^ sw) << 5) + 8 * pp;
    bb[0] = base + 16384 + (64 * ws + c) * 128 + ((q ^ ((c >> 1) & 7)) << 4);
    bb[1] = bb[0] ^ 64;
    f32x4 acc[4][4] = {};
    XSet s0, s1;
#pragma unroll
    for (int st = 0; st < 10; ++st) {
        if (st == 2 && wave == 0 && lane == 0) put_ctl(ep);
        SB(); barn(); SB();
        if (st == 0) x_phase<false, true>(acc, s1, s0, ab, bb[0], xslot(st), xslot(st));
        else x_phase<true, true>(acc, s1, s0, ab, bb[0], xslot(st), xslot(st));
        x_phase<true, true>(acc, s0, s1, ab, bb[1], xslot(st) + 8192, xslot(st));
    }
    x_phase<true, false>(acc, s1, s0, ab, 0, 0, 0);
    const rsrc_t rw = mk(p.ws, WS_BYTES);
#pragma unroll
    for (int ebl = 0; ebl < 2; ++ebl)
#pragma unroll
        for (int u = 0; u < 4; ++u) {
            u32x4 v;
            v[0] = pk(acc[2 * ebl][u][0], acc[2 * ebl][u][1]);
            v[1] = pk(acc[2 * ebl][u][2], acc[2 * ebl][u][3]);
            v[2] = pk(acc[2 * ebl + 1][u][0], acc[2 * ebl + 1][u][1]);
            v[3] = pk(acc[2 * ebl + 1][u][2], acc[2 * ebl + 1][u][3]);
            const u32 off = XF_OFF + ((sb * 20 + 4 * eblk + 2 * we + ebl) * 8 + 4 * ws + u) * 1024;
            __builtin_amdgcn_raw_buffer_store_b128(v, rw, lane * 16, off, 16);
        }
}

constexpr u32 YQ = 1552, YEB = 4 * YQ;
__device__ __forceinline__ u32 add2(u32 a, u32 b) {
    const float lo = fadd(__uint_as_float(a << 16), __uint_as_float(b << 16));
    const float hi = fadd(__uint_as_float(a & 0xffff0000u), __uint_as_float(b & 0xffff0000u));
    return pk(lo, hi);
}
struct YCtx { u32 voff, wa[3]; rsrc_t rw; };
template <int P> struct YP {
    static constexpr int ST = P / 3, J = P % 3;
    static __device__ __forceinline__ void issue(u32x4 (&r)[YLA][2], const YCtx& c) {
        gld_ho(r[P % YLA][0], c.voff, c.rw, (ST * 12 + J) * 1024);
        gld_ho(r[P % YLA][1], c.voff, c.rw, (ST * 12 + J) * 1024 + 16 * 120 * 1024);
    }
    static __device__ __forceinline__ void pro(u32x4 (&r)[YLA][2], const YCtx& c) {
        issue(r, c);
        if constexpr (P + 1 < YLA) YP<P + 1>::pro(r, c);
    }
    static __device__ __forceinline__ void run(u32x4 (&r)[YLA][2], const YCtx& c, Stamps& stp) {
        constexpr int REM = (29 - P) < (YLA - 1) ? (29 - P) : (YLA - 1);
        waitv2<REM * 2>(r[P % YLA][0], r[P % YLA][1]);
        u32x4 o;
#pragma unroll
        for (int e = 0; e < 4; ++e) o[e] = add2(r[P % YLA][0][e], r[P % YLA][1][e]);
        *(LAS u32x4*)(c.wa[J] + ST * 2 * YEB) = o;
        if constexpr (P + YLA < 30) YP<P + YLA>::issue(r, c);
        if constexpr (J == 2) { barw(); STAMP(20 + ST); }
        if constexpr (P + 1 < 30) YP<P + 1>::run(r, c, stp);
    }
};

__device__ __forceinline__ void y_load(const Params& p, int pw, int lane, int gb, u64 tag, bool updater, Stamps& stp) {
    const u32 base = (u32)(uintptr_t)smem;
    const int q = lane >> 4, c = lane & 15;
    YCtx cx;
    cx.rw = mk(p.ws, WS_BYTES);
    cx.voff = MF_OFF + (gb * 120 + 3 * pw) * 1024 + lane * 16;
#pragma unroll
    for (int j = 0; j < 3; ++j) {
        const int ci = 3 * pw + j;
        cx.wa[j] = base + (ci / 6) * YEB + q * YQ + (16 * (ci % 6) + c) * 16;
    }
    const u64* fl = (const u64*)((const char*)p.ws + FL_OFF);
    wait_ready(pw == 0, fl + (gb >> 1) * 20 + lane, lane < 20, tag, 16);
    STAMP(19);
    u32x4 r[YLA][2];
    YP<0>::pro(r, cx);
    YP<0>::run(r, cx, stp);
    if (updater && pw == 0) {
#pragma unroll 1
        for (u32 spins = 0; spins < (1u << 16); ++spins) {
            u32x4 f0, f1;
            gld_ho(f0, FL_OFF + lane * 32, cx.rw, 0);
            gld_ho(f1, FL_OFF + lane * 32 + 16, cx.rw, 0);
            waitv2<0>(f0, f1);
            const u32 tl = (u32)tag, th = (u32)(tag >> 32);
            const bool ok = f0[0] == tl && f0[1] == th && f0[2] == tl && f0[3] == th && f1[0] == tl && f1[1] == th && f1[2] == tl && f1[3] == th;
            if (__all(ok)) break;
            __builtin_amdgcn_s_sleep(1);
        }
        if (lane == 0) __hip_atomic_store((u64*)((char*)p.ws + EP_OFF), tag, __ATOMIC_RELAXED, __HIP_MEMORY_SCOPE_AGENT);
    }
}

__device__ __forceinline__ void y_mma(const Params& p, int w, int lane, int sb, int gb, u64 tag, Stamps& stp) {
    const u32 base = (u32)(uintptr_t)smem;
    const int q = lane >> 4, c = lane & 15;
    const rsrc_t rw = mk(p.ws, WS_BYTES);
    const u64* fl = (const u64*)((const char*)p.ws + FL_OFF);
    wait_ready(w == 0, fl + 160 + sb * 5 + lane, lane < 5, tag, 20);
    STAMP(18);
    bf16x8 xa[20][2];
#pragma unroll
    for (int eb = 0; eb < 20; ++eb) {
#pragma unroll
        for (int u = 0; u < 2; ++u)
            xa[eb][u] = __builtin_bit_cast(bf16x8, __builtin_amdgcn_raw_buffer_load_b128(rw, XF_OFF + (sb * 160 + 2 * w) * 1024 + lane * 16, (eb * 8 + u) * 1024, 16));
        if (2 * eb + 2 > XTH) waitv<XTH>();
    }
    STAMP(19);
    const u32 bb = base + q * YQ + c * 96;
    f32x4 acc[2][6] = {};
    bf16x8 b[YR];
    SB(); barn(); SB();
#pragma unroll
    for (int f = 0; f < YR; ++f) b[f] = ldsr128(bb + (f / 6) * YEB + (f % 6) * 16);
    SB();
#pragma unroll
    for (int f = 0; f < 120; ++f) {
        const int eb = f / 6, n = f % 6;
        acc[0][n] = MFMA(xa[eb][0], b[f % YR], acc[0][n]);
        acc[1][n] = MFMA(xa[eb][1], b[f % YR], acc[1][n]);
        SB();
        if (f + YR < 120) {
            if ((f + YR) % 12 == 0) { barn(); SB(); }
            b[f % YR] = ldsr128(bb + ((f + YR) / 6) * YEB + ((f + YR) % 6) * 16);
            SB();
        }
    }
    STAMP(30);
    const rsrc_t ry = mk(p.y, S_ * F_ * 2);
    const u32 yo = ((sb * 128 + 32 * w + 4 * q) * F_ + gb * 96 + 6 * c) * 2;
#pragma unroll
    for (int u = 0; u < 2; ++u)
#pragma unroll
        for (int r = 0; r < 4; ++r) {
            u32x3 v;
            v[0] = pk(acc[u][0][r], acc[u][1][r]);
            v[1] = pk(acc[u][2][r], acc[u][3][r]);
            v[2] = pk(acc[u][4][r], acc[u][5][r]);
            __builtin_amdgcn_raw_buffer_store_b96(v, ry, yo, (16 * u + r) * F_ * 2, 2);
        }
    asm volatile(".Lqk_end:");
}

__global__ __launch_bounds__(512, 2) void quip_kernel(Params p) {
    const int b = blockIdx.x, xcd = b & 7, i = b >> 3;
    const int lane = threadIdx.x & 63;
    const int wave = __builtin_amdgcn_readfirstlane(threadIdx.x >> 6);
    Stamps stp;
    u32 cpf = 0;
    if (wave == CPFW) {
        u64 pc; u32 len;
        asm volatile("s_getpc_b64 %0\n.Lqk_start:\n\ts_mov_b32 %1, .Lqk_end-.Lqk_start" : "=s"(pc), "=s"(len));
        const rsrc_t rc = mk((const void*)pc, len);
        asm volatile("buffer_load_dword %0, %1, %2, 0 offen" : "=v"(cpf) : "v"(((i % CPFN) * 64 + lane) * 128), "s"(rc) : "memory");
    }
#pragma unroll
    for (int k = 0; k < 32; ++k) stp.t[k] = 0;
    STAMP(0);
    if (wave == 0 && lane == 0) __hip_atomic_store((u32*)((char*)p.ws + (16u << 20)) + b, stp.t[0], __ATOMIC_RELAXED, __HIP_MEMORY_SCOPE_AGENT);
    const int sb2 = 8 * (xcd & 1) + (i & 7), gb2 = 4 * (xcd >> 1) + (i >> 3);
    const int half = xcd & 1, et = 5 * ((xcd >> 1) & 1) + i % 5, gt = 4 * (xcd >> 2) + i / 5;
    const int j = i - 20, sb = 2 * xcd + j / 5, eblk = j % 5;
    if (wave >= 4) {
        if (i < 20) m_prod(p, wave - 4, lane, et, gt, half, stp);
        else if (i < 30) x_prod(p, wave - 4, lane, sb, eblk, stp);
        else barn();
        STAMP(15);
        y_mma(p, wave - 4, lane, sb2, gb2, get_tag(), stp);
    } else {
        int fidx;
        if (i < 20) { m_cons(p, wave, lane, et, gt, half, stp); fidx = gt * 20 + et * 2 + half; }
        else if (i < 30) { x_cons(p, wave, lane, sb, eblk, stp); fidx = 160 + sb * 5 + eblk; }
        else {
            if (wave == 0 && lane == 0) put_ctl(ld_epoch(p));
            barw();
            fidx = b;
        }
        STAMP(15);
        const u64 tag = get_tag();
        publish(p, lane, fidx, tag);
        asm volatile("" :: "v"(cpf));
        STAMP(17);
        y_load(p, wave, lane, gb2, tag, b == 255, stp);
    }
    waitv<0>();
    STAMP(31);
    {
        u32 d = 0;
        const u32 tg = (u32)get_tag();
        if (AMP_SEL == 102 && wave >= 4 && lane == 0)
            __hip_atomic_store((u64*)((char*)p.ws + (16u << 20) + 4096) + b * 4 + wave - 4, ((u64)tg << 32) | stp.t[31], __ATOMIC_RELAXED, __HIP_MEMORY_SCOPE_AGENT);
        if (AMP_SEL >= 100) {
            if (b == 255 && wave == 0) {
                const rsrc_t rw = mk(p.ws, 32u << 20);
                u32x4 v; gld_ho(v, (16u << 20) + lane * 16, rw, 0); waitv1<0>(v);
                const u32 t0 = stp.t[0];
                int mn = min(min((int)(v[0] - t0), (int)(v[1] - t0)), min((int)(v[2] - t0), (int)(v[3] - t0)));
                int mx = max(max((int)(v[0] - t0), (int)(v[1] - t0)), max((int)(v[2] - t0), (int)(v[3] - t0)));
                for (int o = 32; o; o >>= 1) { mn = min(mn, __shfl_xor(mn, o)); mx = max(mx, __shfl_xor(mx, o)); }
                mn = __builtin_amdgcn_readfirstlane(mn); mx = __builtin_amdgcn_readfirstlane(mx);
                d = AMP_SEL == 100 ? (u32)((int)(stp.t[31] - t0) - mn) : AMP_SEL == 101 ? (u32)(mx - mn) : 200;
                if (AMP_SEL == 102) {
                    int me = 0;
                    for (int k = 0; k < 8; ++k) {
                        for (u32 spins = 0; spins < 100000; ++spins) {
                            gld_ho(v, (16u << 20) + 4096 + k * 1024 + lane * 16, rw, 0); waitv1<0>(v);
                            if (__all(v[1] == tg && v[3] == tg)) break;
                        }
                        me = max(me, max((int)(v[0] - t0), (int)(v[2] - t0)));
                    }
                    for (int o = 32; o; o >>= 1) me = max(me, __shfl_xor(me, o));
                    me = __builtin_amdgcn_readfirstlane(me);
                    d = me - mn;
                }
                const u32 ta = __builtin_amdgcn_readfirstlane(*(const volatile u32*)((const char*)p.ws + (16u << 20) + 65536));
                const u32 tgt = ta + AMP_BASE + (t0 + mn - ta) * AMP_K;
                while ((int)((u32)__builtin_amdgcn_s_memrealtime() - tgt) < 0) __builtin_amdgcn_s_sleep(4);
            }
        } else if (AMP_SEL >= 0) {
            if (b == AMP_B && wave == AMP_W) {
                d = stp.t[AMP_SEL < 0 ? 0 : AMP_SEL % 100] - stp.t[0];
                const u32 tgt = stp.t[0] + AMP_BASE + d * AMP_K;
                while ((int)((u32)__builtin_amdgcn_s_memrealtime() - tgt) < 0) __builtin_amdgcn_s_sleep(4);
            }
        }
    }
}

__global__ void pre_kernel(Params p) {
    u64 t_; asm volatile("s_memrealtime %0\n\ts_waitcnt lgkmcnt(0)" : "=s"(t_) :: "memory");
    if (blockIdx.x == 0 && threadIdx.x == 0) *(u32*)((char*)p.ws + (16u << 20) + 65536) = (u32)t_;
}
extern "C" void kernel_launch(void* const* d_in, const int* in_sizes, int n_in, void* d_out, int out_size, void* d_ws,
                              size_t ws_size, hipStream_t stream) {
    Params p;
    p.x = d_in[0]; p.qw = d_in[1]; p.sc = d_in[2]; p.ze = d_in[3]; p.U = d_in[4]; p.V = d_in[5];
    p.y = d_out; p.ws = d_ws;
    pre_kernel<<<PRE_G, PRE_B, 0, stream>>>(p);
    quip_kernel<<<256, 512, 0, stream>>>(p);
}
```

```cpp
#define PROF 1
#define AMP_SEL 18
#define AMP_B 8
#define AMP_W 4
#define AMP_BASE 4000
#define AMP_K 5
#include <hip/hip_runtime.h>
#include <stdint.h>
#pragma clang diagnostic ignored "-Wint-to-pointer-cast"

#define MLA 20
#define XA 2
#define YLA 8
#define YR 6
#define XTH 8

#define CPFW 3
#define CPFN 7
typedef unsigned u32;
typedef unsigned long long u64;
using bf16x8 = __attribute__((ext_vector_type(8))) short;
using s16x4 = __attribute__((ext_vector_type(4))) short;
using f32x4 = __attribute__((ext_vector_type(4))) float;
using u32x4 = __attribute__((ext_vector_type(4))) unsigned;
using u32x3 = __attribute__((ext_vector_type(3))) unsigned;
using u32x2 = __attribute__((ext_vector_type(2))) unsigned;
using bf16x2_t = __attribute__((ext_vector_type(2))) __bf16;
using f32x2 = __attribute__((ext_vector_type(2))) float;
#define LAS __attribute__((address_space(3)))
typedef __amdgpu_buffer_rsrc_t rsrc_t;

constexpr int S_ = 2048, D_ = 640, F_ = 1536;
constexpr u32 XF_OFF = 0, XF_BYTES = S_ * D_ * 2;
constexpr u32 MF_OFF = XF_BYTES, MF_BYTES = 2 * D_ * F_ * 2;
constexpr u32 FL_OFF = MF_OFF + MF_BYTES;
constexpr u32 EP_OFF = FL_OFF + 4096;
constexpr u32 WS_BYTES = EP_OFF + 256;
constexpr int LDS_BYTES = 163840;
constexpr u32 CTL_OFF = 162816;
constexpr u32 TOP_OFF = 129024;
constexpr u64 TAGC = 0x9E3779B97F4A7C15ull;

struct Params {
    const void *x, *qw, *sc, *ze, *U, *V;
    void *y, *ws;
};

__device__ __forceinline__ rsrc_t mk(const void* p, u32 bytes) { return __builtin_amdgcn_make_buffer_rsrc((void*)p, (short)0, (int)bytes, 0x00020000); }
__device__ __forceinline__ void glds(u32 m0v, u32 voff, rsrc_t r, u32 soff) {
    asm volatile("s_mov_b32 m0, %0\n\ts_nop 0\n\tbuffer_load_dwordx4 %1, %2, %3 offen lds" ::"s"(m0v), "v"(voff), "s"(r), "s"(soff) : "memory");
}
__device__ __forceinline__ void gld(u32x4& d, u32 voff, rsrc_t r, u32 soff) {
    asm volatile("buffer_load_dwordx4 %0, %1, %2, %3 offen" : "=v"(d) : "v"(voff), "s"(r), "s"(soff) : "memory");
}
__device__ __forceinline__ void gld_ho(u32x4& d, u32 voff, rsrc_t r, u32 soff) {
    asm volatile("buffer_load_dwordx4 %0, %1, %2, %3 offen sc1" : "=v"(d) : "v"(voff), "s"(r), "s"(soff) : "memory");
}
__device__ __forceinline__ void gld_ho2(u32x2& d, u32 voff, rsrc_t r, u32 soff) {
    asm volatile("buffer_load_dwordx2 %0, %1, %2, %3 offen sc1" : "=v"(d) : "v"(voff), "s"(r), "s"(soff) : "memory");
}
template <int N> __device__ __forceinline__ void waitv() { asm volatile("s_waitcnt vmcnt(%0)" ::"n"(N) : "memory"); }
template <int N, class T> __device__ __forceinline__ void waitv1(T& a) { asm volatile("s_waitcnt vmcnt(%1)" : "+v"(a) : "n"(N) : "memory"); }
template <int N, class T> __device__ __forceinline__ void waitv2(T& a, T& b) { asm volatile("s_waitcnt vmcnt(%2)" : "+v"(a), "+v"(b) : "n"(N) : "memory"); }
__device__ __forceinline__ void barw() { asm volatile("s_waitcnt lgkmcnt(0)\n\ts_barrier" ::: "memory"); }
__device__ __forceinline__ void barn() { asm volatile("s_barrier" ::: "memory"); }
#define SB() __builtin_amdgcn_sched_barrier(0)
__device__ __forceinline__ u32 pk(float a, float b) {
    f32x2 v = {a, b};
    return __builtin_bit_cast(u32, __builtin_convertvector(v, bf16x2_t));
}
__device__ __forceinline__ float fadd(float a, float b) { float d; asm("v_add_f32 %0, %1, %2" : "=v"(d) : "v"(a), "v"(b)); return d; }
__device__ __forceinline__ float ffma(float a, float b, float c) { float d; asm("v_fma_f32 %0, %1, %2, %3" : "=v"(d) : "v"(a), "v"(b), "v"(c)); return d; }
__device__ __forceinline__ bf16x8 ldsr128(u32 a) { return *(const LAS bf16x8*)a; }
__device__ __forceinline__ s16x4 ldstr(u32 a) { return __builtin_amdgcn_ds_read_tr16_b64_v4i16((LAS s16x4*)a); }
__device__ __forceinline__ bf16x8 cat(s16x4 a, s16x4 b) { return __builtin_shufflevector(a, b, 0, 1, 2, 3, 4, 5, 6, 7); }
#define MFMA(a, b, c) __builtin_amdgcn_mfma_f32_16x16x32_bf16(a, b, c, 0, 0, 0)

__shared__ __attribute__((aligned(1024))) char smem[LDS_BYTES];

struct Stamps { u32 t[32]; };
#define STAMP(k) do { u64 t_; asm volatile("s_memrealtime %0\n\ts_waitcnt lgkmcnt(0)" : "=s"(t_) :: "memory"); stp.t[k] = (u32)t_ | 1u; } while (0)
#define PROF_ROLE(b) (((b) >> 3) < 20 ? 0 : ((b) >> 3) < 30 ? 1 : 2)
#define PROF_NB 256

__device__ __forceinline__ u64 ld_epoch(const Params& p) {
    return __hip_atomic_load((const u64*)((const char*)p.ws + EP_OFF), __ATOMIC_RELAXED, __HIP_MEMORY_SCOPE_AGENT);
}
__device__ __forceinline__ void put_ctl(u64 e) {
    const u32 base = (u32)(uintptr_t)smem;
    *(LAS u64*)(base + CTL_OFF) = e + TAGC;
    *(LAS u32*)(base + CTL_OFF + 8) = 0;
    *(LAS u32x2*)(base + CTL_OFF + 16) = u32x2{0, 0};
}
__device__ __forceinline__ u64 get_tag() { return *(const LAS u64*)((u32)(uintptr_t)smem + CTL_OFF); }
__device__ __forceinline__ void publish(const Params& p, int lane, int fidx, u64 tag) {
    waitv<0>();
    if (lane == 0) {
        const u32 old = __hip_atomic_fetch_add((LAS u32*)((u32)(uintptr_t)smem + CTL_OFF + 8), 1u, __ATOMIC_RELAXED, __HIP_MEMORY_SCOPE_WORKGROUP);
        if (old == 3) __hip_atomic_store((u64*)((char*)p.ws + FL_OFF) + fidx, tag, __ATOMIC_RELAXED, __HIP_MEMORY_SCOPE_AGENT);
    }
}
__device__ __forceinline__ void poll(const u64* f, bool active, u64 tag) {
    for (u32 spins = 0; spins < (1u << 16); ++spins) {
        u64 v = tag;
        if (active) v = __hip_atomic_load(f, __ATOMIC_RELAXED, __HIP_MEMORY_SCOPE_AGENT);
        if (__all(v == tag)) return;
        __builtin_amdgcn_s_sleep(1);
    }
}

__device__ __forceinline__ void wait_ready(bool leader, const u64* f, bool active, u64 tag, u32 word) {
    LAS u32* w = (LAS u32*)((u32)(uintptr_t)smem + CTL_OFF + word);
    if (leader) {
        poll(f, active, tag);
        __hip_atomic_store(w, 1u, __ATOMIC_RELAXED, __HIP_MEMORY_SCOPE_WORKGROUP);
    } else {
        for (u32 spins = 0; spins < (1u << 22); ++spins) {
            if (__builtin_amdgcn_readfirstlane(__hip_atomic_load(w, __ATOMIC_RELAXED, __HIP_MEMORY_SCOPE_WORKGROUP))) break;
            __builtin_amdgcn_s_sleep(1);
        }
    }
    asm volatile("" ::: "memory");
}

constexpr u32 mbslot(int st) { return st % 4 == 3 ? TOP_OFF : (st % 4) * 24576; }
constexpr u32 maslot(int st) { return st % 3 == 2 ? TOP_OFF + 24576 : 73728 + (st % 3) * 8192; }
constexpr u32 MS_OFF = 90112, MZ_OFF = MS_OFF + 3072;
static_assert(MLA <= 20, "slot reuse");
#define MROT 3
constexpr int MAR = 8;
struct MCtx { u32 voffA, voffB[6], wa[4], wb, sca; rsrc_t rq, rv; int rot; };
__device__ __forceinline__ u32 krot(int st, int rot) {
    if (MROT == 0) return st;
    const int k = st + rot;
    return k >= 12 ? k - 12 : k;
}
#define MORD 0
constexpr int mord(int k) {
    constexpr int o0[10] = {0, ~0, 1, ~1, 2, 3, ~2, 4, ~3, 5};
    constexpr int o1[10] = {~0, 0, ~1, 1, ~2, 2, ~3, 3, 4, 5};
    constexpr int o2[10] = {~0, ~1, ~2, ~3, 0, 1, 2, 3, 4, 5};
    return MORD == 0 ? o0[k] : MORD == 1 ? o1[k] : o2[k];
}
template <int P> struct MP {
    static constexpr int ST = P / 10, K = P % 10;
    static constexpr bool isA = mord(K) < 0;
    static constexpr int idx = isA ? ~mord(K) : mord(K);
    static constexpr int ar = (ST * 4 + idx) % MAR;
    static __device__ __forceinline__ void issue(u32x4 (&r)[MAR], const MCtx& c) {
        if constexpr (isA) gld(r[ar], c.voffA, c.rq, krot(ST, c.rot) * 256 + idx * 24576);
        else glds(c.wb + mbslot(ST) + idx * 1024, c.voffB[idx], c.rv, krot(ST, c.rot) * 196608);
    }
    static __device__ __forceinline__ void pro(u32x4 (&r)[MAR], const MCtx& c) {
        issue(r, c);
        if constexpr (P + 1 < MLA) MP<P + 1>::pro(r, c);
    }
    static __device__ __forceinline__ void run(u32x4 (&r)[MAR], const MCtx& c, f32x4& s4, f32x4& z4, Stamps& stp) {
        constexpr int REM = (119 - P) < (MLA - 1) ? (119 - P) : (MLA - 1);
        if constexpr (K == 0) {
            s4 = *(const LAS f32x4*)(c.sca + MS_OFF + krot(ST, c.rot) * 256);
            z4 = *(const LAS f32x4*)(c.sca + MZ_OFF + krot(ST, c.rot) * 256);
        }
        if constexpr (isA) {
            waitv1<REM>(r[ar]);
            const u32x4 q = r[ar];
            u32x2 o;
            o[0] = pk(ffma((float)(int)q[0], s4[0], z4[0]), ffma((float)(int)q[1], s4[1], z4[1]));
            o[1] = pk(ffma((float)(int)q[2], s4[2], z4[2]), ffma((float)(int)q[3], s4[3], z4[3]));
            *(LAS u32x2*)(c.wa[idx] + maslot(ST)) = o;
        } else if constexpr (K == 9) {
            waitv<REM>();
        }
        if constexpr (K == 9) { barw(); STAMP(2 + ST); }
        if constexpr (P + MLA < 120) MP<P + MLA>::issue(r, c);
        if constexpr (P + 1 < 120) MP<P + 1>::run(r, c, s4, z4, stp);
    }
};

struct MSet { bf16x8 a[4]; s16x4 bl[3], bh[3]; };
template <bool DOM, bool DOR>
__device__ __forceinline__ void m_phase(f32x4 (&acc)[4][3], const MSet& m, MSet& r, u32 aa, const u32 (&bb)[3], u32 aoff, u32 boff) {
#pragma unroll
    for (int i = 0; i < 12; ++i) {
        if (DOM) acc[i / 3][i % 3] = MFMA(m.a[i / 3], cat(m.bl[i % 3], m.bh[i % 3]), acc[i / 3][i % 3]);
        if (DOR) {
            if (i == 0) r.bl[0] = ldstr(bb[0] + boff);
            if (i == 1) r.bh[0] = ldstr(bb[0] + boff + 1536);
            if (i == 2) r.a[0] = ldsr128(aa + aoff);
            if (i == 3) r.bl[1] = ldstr(bb[1] + boff);
            if (i == 4) r.bh[1] = ldstr(bb[1] + boff + 1536);
            if (i == 5) r.bl[2] = ldstr(bb[2] + boff);
            if (i == 6) r.bh[2] = ldstr(bb[2] + boff + 1536);
            if (i >= 7 && i < 10) r.a[i - 6] = ldsr128(aa + aoff + (i - 6) * 2048);
        }
        SB();
    }
}

__device__ __forceinline__ void m_prod(const Params& p, int pw, int lane, int et, int gt, int half, Stamps& stp) {
    const u32 base = (u32)(uintptr_t)smem;
    const int q = lane >> 4, c = lane & 15;
    const int kb = half * 768;
    MCtx cx;
    cx.rq = mk(p.qw, D_ * F_ * 4); cx.rv = mk(p.V, F_ * F_ * 2);
    cx.voffA = ((et * 64 + 16 * pw + q) * F_ + kb + 4 * c) * 4;
#pragma unroll
    for (int j = 0; j < 6; ++j) {
        const int P = (6 * pw + j) * 1024 + 16 * lane;
        const int r = P / 384, o = P - r * 384;
        const int sw = ((r >> 1) & 1) | (((r >> 3) & 1) << 1);
        cx.voffB[j] = (kb + r) * (F_ * 2) + gt * 384 + (((o >> 5) ^ sw) << 5) + (o & 16);
    }
#pragma unroll
    for (int i = 0; i < 4; ++i) {
        const int row = 16 * pw + 4 * i + q;
        cx.wa[i] = base + row * 128 + (((c >> 1) ^ ((row >> 1) & 7)) << 4) + (c & 1) * 8;
    }
    cx.wb = base + pw * 6144;
    cx.sca = base + c * 16;
    cx.rot = MROT == 1 ? et % 5 : MROT == 2 ? 2 * (et % 5) : MROT == 3 ? ((et % 5) + 3 * (gt & 3)) % 12 : MROT == 4 ? (gt & 3) : 0;
    u32x4 r[MAR];
    MP<0>::pro(r, cx);
    STAMP(1);
    barn();
    f32x4 s4, z4;
    MP<0>::run(r, cx, s4, z4, stp);
}

__device__ __forceinline__ void m_cons(const Params& p, int wave, int lane, int et, int gt, int half, Stamps& stp) {
    const u32 base = (u32)(uintptr_t)smem;
    const int q = lane >> 4, c = lane & 15;
    const int kb = half * 768;
    {
        const int t = wave * 64 + lane;
        if (t < 192) {
            const u32x2 sv = *(const u32x2*)((const char*)p.sc + (kb + 4 * t) * 2);
            const u32x2 zv = *(const u32x2*)((const char*)p.ze + (kb + 4 * t) * 2);
            f32x4 s4, z4;
            s4[0] = __uint_as_float(sv[0] << 16); s4[1] = __uint_as_float(sv[0] & 0xffff0000u);
            s4[2] = __uint_as_float(sv[1] << 16); s4[3] = __uint_as_float(sv[1] & 0xffff0000u);
            z4[0] = -__uint_as_float(zv[0] << 16) * s4[0]; z4[1] = -__uint_as_float(zv[0] & 0xffff0000u) * s4[1];
            z4[2] = -__uint_as_float(zv[1] << 16) * s4[2]; z4[3] = -__uint_as_float(zv[1] & 0xffff0000u) * s4[3];
            *(LAS f32x4*)(base + MS_OFF + t * 16) = s4;
            *(LAS f32x4*)(base + MZ_OFF + t * 16) = z4;
        } else if (t == 192) {
            put_ctl(ld_epoch(p));
        }
    }
    barw();
    STAMP(1);
    const int qq = c >> 2, pp = c & 3;
    const int sw = (qq >> 1) | ((q & 1) << 1);
    u32 ab[2], bb[3];
    ab[0] = base + c * 128 + ((q ^ ((c >> 1) & 7)) << 4);
    ab[1] = ab[0] ^ 64;
#pragma unroll
    for (int n = 0; n < 3; ++n) bb[n] = base + (8 * q + qq) * 384 + (((3 * wave + n) ^ sw) << 5) + 8 * pp;
    f32x4 acc[4][3] = {};
    MSet s0, s1;
#pragma unroll
    for (int st = 0; st < 12; ++st) {
        SB(); barn(); SB();
        if (st == 0) m_phase<false, true>(acc, s1, s0, ab[0], bb, maslot(st), mbslot(st));
        else m_phase<true, true>(acc, s1, s0, ab[0], bb, maslot(st), mbslot(st));
        m_phase<true, true>(acc, s0, s1, ab[1], bb, maslot(st), mbslot(st) + 12288);
    }
    m_phase<true, false>(acc, s1, s0, 0, bb, 0, 0);
    const rsrc_t rw = mk(p.ws, WS_BYTES);
#pragma unroll
    for (int ebl = 0; ebl < 2; ++ebl)
#pragma unroll
        for (int n = 0; n < 3; ++n) {
            const int nt = 3 * wave + n;
            const int gb = 2 * gt + nt / 6, np = nt % 6;
            u32x4 v;
            v[0] = pk(acc[2 * ebl][n][0], acc[2 * ebl][n][1]);
            v[1] = pk(acc[2 * ebl][n][2], acc[2 * ebl][n][3]);
            v[2] = pk(acc[2 * ebl + 1][n][0], acc[2 * ebl + 1][n][1]);
            v[3] = pk(acc[2 * ebl + 1][n][2], acc[2 * ebl + 1][n][3]);
            const u32 off = MF_OFF + (((half * 16 + gb) * 20 + 2 * et + ebl) * 6 + np) * 1024;
            __builtin_amdgcn_raw_buffer_store_b128(v, rw, lane * 16, off, 16);
        }
}

constexpr u32 xslot(int st) { return st % 4 == 1 ? TOP_OFF : st % 4 == 0 ? 0 : (st % 4 - 1) * 32768; }
#define XROT 0
struct XCtx { u32 vU[2], vX[2], ldsb; rsrc_t ru, rx; int rot; };
template <int ST> struct XP {
    static __device__ __forceinline__ void issue(const XCtx& c) {
        int k = ST;
        if (XROT) { k = ST + c.rot; k = k >= 10 ? k - 10 : k; }
#pragma unroll
        for (int j = 0; j < 4; ++j) {
            glds(c.ldsb + xslot(ST) + 16384 + j * 1024, c.vX[j & 1], c.rx, k * 128 + j * 8 * 1280);
            glds(c.ldsb + xslot(ST) + j * 1024, c.vU[j >> 1], c.ru, k * 64 * 1280 + j * 4 * 1280);
        }
    }
    static __device__ __forceinline__ void pro(const XCtx& c) {
        issue(c);
        if constexpr (ST + 1 < XA) XP<ST + 1>::pro(c);
    }
    static __device__ __forceinline__ void run(const XCtx& c, Stamps& stp) {
        constexpr int REM = (9 - ST) < (XA - 1) ? (9 - ST) : (XA - 1);
        waitv<REM * 8>();
        barn();
        STAMP(2 + ST);
        if constexpr (ST + XA < 10) XP<ST + XA>::issue(c);
        if constexpr (ST + 1 < 10) XP<ST + 1>::run(c, stp);
    }
};
static_assert(XA <= 2, "slot reuse");

struct XSet { s16x4 al[4], ah[4]; bf16x8 b[4]; };
template <bool DOM, bool DOR>
__device__ __forceinline__ void x_phase(f32x4 (&acc)[4][4], const XSet& m, XSet& r, const u32 (&ab)[4], u32 bb, u32 aoff, u32 boff) {
#pragma unroll
    for (int i = 0; i < 16; ++i) {
        if (DOM) acc[i / 4][i % 4] = MFMA(cat(m.al[i / 4], m.ah[i / 4]), m.b[i % 4], acc[i / 4][i % 4]);
        if (DOR) {
            if (i == 0) r.al[0] = ldstr(ab[0] + aoff);
            if (i == 1) r.ah[0] = ldstr(ab[0] + aoff + 1024);
            if (i >= 2 && i < 6) r.b[i - 2] = ldsr128(bb + boff + (i - 2) * 2048);
            if (i >= 6 && i < 12 && !(i & 1)) r.al[(i - 4) / 2] = ldstr(ab[(i - 4) / 2] + aoff);
            if (i >= 6 && i < 12 && (i & 1)) r.ah[(i - 4) / 2] = ldstr(ab[(i - 4) / 2] + aoff + 1024);
        }
        SB();
    }
}

__device__ __forceinline__ void x_prod(const Params& p, int pw, int lane, int sb, int eblk, Stamps& stp) {
    const u32 base = (u32)(uintptr_t)smem;
    const int q = lane >> 4, c = lane & 15;
    XCtx cx;
    cx.ru = mk(p.U, D_ * D_ * 2); cx.rx = mk(p.x, S_ * D_ * 2);
#pragma unroll
    for (int h = 0; h < 2; ++h) {
        cx.vU[h] = (16 * pw + q) * 1280 + eblk * 256 + (((c >> 1) ^ (q | (h << 2))) << 5) + (c & 1) * 16;
        cx.vX[h] = (sb * 128 + 32 * pw + (lane >> 3)) * 1280 + (((lane & 7) ^ ((4 * h + q) & 7)) << 4);
    }
    cx.ldsb = base + pw * 4096;
    cx.rot = XROT == 1 ? (2 * eblk + 5 * (sb & 1)) % 10 : XROT == 2 ? 2 * eblk : (2 * eblk + (sb & 1)) % 10;
    XP<0>::pro(cx);
    STAMP(1);
    XP<0>::run(cx, stp);
}

__device__ __forceinline__ void x_cons(const Params& p, int wave, int lane, int sb, int eblk, Stamps& stp) {
    const u32 base = (u32)(uintptr_t)smem;
    const int q = lane >> 4, c = lane & 15;
    const int we = wave & 1, ws = wave >> 1;
    const int qq = c >> 2, pp = c & 3;
    const int sw = qq | ((q & 1) << 2);
    u64 ep = 0;
    if (wave == 0 && lane == 0) ep = ld_epoch(p);
    u32 ab[4], bb[2];
#pragma unroll
    for (int t = 0; t < 4; ++t) ab[t] = base + (8 * q + qq) * 256 + (((4 * we + t) ^ sw) << 5) + 8 * pp;
    bb[0] = base + 16384 + (64 * ws + c) * 128 + ((q ^ ((c >> 1) & 7)) << 4);
    bb[1] = bb[0] ^ 64;
    f32x4 acc[4][4] = {};
    XSet s0, s1;
#pragma unroll
    for (int st = 0; st < 10; ++st) {
        if (st == 2 && wave == 0 && lane == 0) put_ctl(ep);
        SB(); barn(); SB();
        if (st == 0) x_phase<false, true>(acc, s1, s0, ab, bb[0], xslot(st), xslot(st));
        else x_phase<true, true>(acc, s1, s0, ab, bb[0], xslot(st), xslot(st));
        x_phase<true, true>(acc, s0, s1, ab, bb[1], xslot(st) + 8192, xslot(st));
    }
    x_phase<true, false>(acc, s1, s0, ab, 0, 0, 0);
    const rsrc_t rw = mk(p.ws, WS_BYTES);
#pragma unroll
    for (int ebl = 0; ebl < 2; ++ebl)
#pragma unroll
        for (int u = 0; u < 4; ++u) {
            u32x4 v;
            v[0] = pk(acc[2 * ebl][u][0], acc[2 * ebl][u][1]);
            v[1] = pk(acc[2 * ebl][u][2], acc[2 * ebl][u][3]);
            v[2] = pk(acc[2 * ebl + 1][u][0], acc[2 * ebl + 1][u][1]);
            v[3] = pk(acc[2 * ebl + 1][u][2], acc[2 * ebl + 1][u][3]);
            const u32 off = XF_OFF + ((sb * 20 + 4 * eblk + 2 * we + ebl) * 8 + 4 * ws + u) * 1024;
            __builtin_amdgcn_raw_buffer_store_b128(v, rw, lane * 16, off, 16);
        }
}

constexpr u32 YQ = 1552, YEB = 4 * YQ;
__device__ __forceinline__ u32 add2(u32 a, u32 b) {
    const float lo = fadd(__uint_as_float(a << 16), __uint_as_float(b << 16));
    const float hi = fadd(__uint_as_float(a & 0xffff0000u), __uint_as_float(b & 0xffff0000u));
    return pk(lo, hi);
}
struct YCtx { u32 voff, wa[3]; rsrc_t rw; };
template <int P> struct YP {
    static constexpr int ST = P / 3, J = P % 3;
    static __device__ __forceinline__ void issue(u32x4 (&r)[YLA][2], const YCtx& c) {
        gld_ho(r[P % YLA][0], c.voff, c.rw, (ST * 12 + J) * 1024);
        gld_ho(r[P % YLA][1], c.voff, c.rw, (ST * 12 + J) * 1024 + 16 * 120 * 1024);
    }
    static __device__ __forceinline__ void pro(u32x4 (&r)[YLA][2], const YCtx& c) {
        issue(r, c);
        if constexpr (P + 1 < YLA) YP<P + 1>::pro(r, c);
    }
    static __device__ __forceinline__ void run(u32x4 (&r)[YLA][2], const YCtx& c, Stamps& stp) {
        constexpr int REM = (29 - P) < (YLA - 1) ? (29 - P) : (YLA - 1);
        waitv2<REM * 2>(r[P % YLA][0], r[P % YLA][1]);
        u32x4 o;
#pragma unroll
        for (int e = 0; e < 4; ++e) o[e] = add2(r[P % YLA][0][e], r[P % YLA][1][e]);
        *(LAS u32x4*)(c.wa[J] + ST * 2 * YEB) = o;
        if constexpr (P + YLA < 30) YP<P + YLA>::issue(r, c);
        if constexpr (J == 2) { barw(); STAMP(20 + ST); }
        if constexpr (P + 1 < 30) YP<P + 1>::run(r, c, stp);
    }
};

__device__ __forceinline__ void y_load(const Params& p, int pw, int lane, int gb, u64 tag, bool updater, Stamps& stp) {
    const u32 base = (u32)(uintptr_t)smem;
    const int q = lane >> 4, c = lane & 15;
    YCtx cx;
    cx.rw = mk(p.ws, WS_BYTES);
    cx.voff = MF_OFF + (gb * 120 + 3 * pw) * 1024 + lane * 16;
#pragma unroll
    for (int j = 0; j < 3; ++j) {
        const int ci = 3 * pw + j;
        cx.wa[j] = base + (ci / 6) * YEB + q * YQ + (16 * (ci % 6) + c) * 16;
    }
    const u64* fl = (const u64*)((const char*)p.ws + FL_OFF);
    wait_ready(pw == 0, fl + (gb >> 1) * 20 + lane, lane < 20, tag, 16);
    STAMP(19);
    u32x4 r[YLA][2];
    YP<0>::pro(r, cx);
    YP<0>::run(r, cx, stp);
    if (updater && pw == 0) {
#pragma unroll 1
        for (u32 spins = 0; spins < (1u << 16); ++spins) {
            u32x4 f0, f1;
            gld_ho(f0, FL_OFF + lane * 32, cx.rw, 0);
            gld_ho(f1, FL_OFF + lane * 32 + 16, cx.rw, 0);
            waitv2<0>(f0, f1);
            const u32 tl = (u32)tag, th = (u32)(tag >> 32);
            const bool ok = f0[0] == tl && f0[1] == th && f0[2] == tl && f0[3] == th && f1[0] == tl && f1[1] == th && f1[2] == tl && f1[3] == th;
            if (__all(ok)) break;
            __builtin_amdgcn_s_sleep(1);
        }
        if (lane == 0) __hip_atomic_store((u64*)((char*)p.ws + EP_OFF), tag, __ATOMIC_RELAXED, __HIP_MEMORY_SCOPE_AGENT);
    }
}

__device__ __forceinline__ void y_mma(const Params& p, int w, int lane, int sb, int gb, u64 tag, Stamps& stp) {
    const u32 base = (u32)(uintptr_t)smem;
    const int q = lane >> 4, c = lane & 15;
    const rsrc_t rw = mk(p.ws, WS_BYTES);
    const u64* fl = (const u64*)((const char*)p.ws + FL_OFF);
    wait_ready(w == 0, fl + 160 + sb * 5 + lane, lane < 5, tag, 20);
    STAMP(18);
    bf16x8 xa[20][2];
#pragma unroll
    for (int eb = 0; eb < 20; ++eb) {
#pragma unroll
        for (int u = 0; u < 2; ++u)
            xa[eb][u] = __builtin_bit_cast(bf16x8, __builtin_amdgcn_raw_buffer_load_b128(rw, XF_OFF + (sb * 160 + 2 * w) * 1024 + lane * 16, (eb * 8 + u) * 1024, 16));
        if (2 * eb + 2 > XTH) waitv<XTH>();
    }
    STAMP(19);
    const u32 bb = base + q * YQ + c * 96;
    f32x4 acc[2][6] = {};
    bf16x8 b[YR];
    SB(); barn(); SB();
#pragma unroll
    for (int f = 0; f < YR; ++f) b[f] = ldsr128(bb + (f / 6) * YEB + (f % 6) * 16);
    SB();
#pragma unroll
    for (int f = 0; f < 120; ++f) {
        const int eb = f / 6, n = f % 6;
        acc[0][n] = MFMA(xa[eb][0], b[f % YR], acc[0][n]);
        acc[1][n] = MFMA(xa[eb][1], b[f % YR], acc[1][n]);
        SB();
        if (f + YR < 120) {
            if ((f + YR) % 12 == 0) { barn(); SB(); }
            b[f % YR] = ldsr128(bb + ((f + YR) / 6) * YEB + ((f + YR) % 6) * 16);
            SB();
        }
    }
    STAMP(30);
    const rsrc_t ry = mk(p.y, S_ * F_ * 2);
    const u32 yo = ((sb * 128 + 32 * w + 4 * q) * F_ + gb * 96 + 6 * c) * 2;
#pragma unroll
    for (int u = 0; u < 2; ++u)
#pragma unroll
        for (int r = 0; r < 4; ++r) {
            u32x3 v;
            v[0] = pk(acc[u][0][r], acc[u][1][r]);
            v[1] = pk(acc[u][2][r], acc[u][3][r]);
            v[2] = pk(acc[u][4][r], acc[u][5][r]);
            __builtin_amdgcn_raw_buffer_store_b96(v, ry, yo, (16 * u + r) * F_ * 2, 2);
        }
    asm volatile(".Lqk_end:");
}

__global__ __launch_bounds__(512, 2) void quip_kernel(Params p) {
    const int b = blockIdx.x, xcd = b & 7, i = b >> 3;
    const int lane = threadIdx.x & 63;
    const int wave = __builtin_amdgcn_readfirstlane(threadIdx.x >> 6);
    Stamps stp;
    u32 cpf = 0;
    if (wave == CPFW) {
        u64 pc; u32 len;
        asm volatile("s_getpc_b64 %0\n.Lqk_start:\n\ts_mov_b32 %1, .Lqk_end-.Lqk_start" : "=s"(pc), "=s"(len));
        const rsrc_t rc = mk((const void*)pc, len);
        asm volatile("buffer_load_dword %0, %1, %2, 0 offen" : "=v"(cpf) : "v"(((i % CPFN) * 64 + lane) * 128), "s"(rc) : "memory");
    }
#pragma unroll
    for (int k = 0; k < 32; ++k) stp.t[k] = 0;
    STAMP(0);
    if (wave == 0 && lane == 0) __hip_atomic_store((u32*)((char*)p.ws + (16u << 20)) + b, stp.t[0], __ATOMIC_RELAXED, __HIP_MEMORY_SCOPE_AGENT);
    const int sb2 = 8 * (xcd & 1) + (i & 7), gb2 = 4 * (xcd >> 1) + (i >> 3);
    const int half = xcd & 1, et = 5 * ((xcd >> 1) & 1) + i % 5, gt = 4 * (xcd >> 2) + i / 5;
    const int j = i - 20, sb = 2 * xcd + j / 5, eblk = j % 5;
    if (wave >= 4) {
        if (i < 20) m_prod(p, wave - 4, lane, et, gt, half, stp);
        else if (i < 30) x_prod(p, wave - 4, lane, sb, eblk, stp);
        else barn();
        STAMP(15);
        y_mma(p, wave - 4, lane, sb2, gb2, get_tag(), stp);
    } else {
        int fidx;
        if (i < 20) { m_cons(p, wave, lane, et, gt, half, stp); fidx = gt * 20 + et * 2 + half; }
        else if (i < 30) { x_cons(p, wave, lane, sb, eblk, stp); fidx = 160 + sb * 5 + eblk; }
        else {
            if (wave == 0 && lane == 0) put_ctl(ld_epoch(p));
            barw();
            fidx = b;
        }
        STAMP(15);
        const u64 tag = get_tag();
        publish(p, lane, fidx, tag);
        asm volatile("" :: "v"(cpf));
        STAMP(17);
        y_load(p, wave, lane, gb2, tag, b == 255, stp);
    }
    waitv<0>();
    STAMP(31);
    {
        u32 d = 0;
        const u32 tg = (u32)get_tag();
        if (AMP_SEL == 102 && wave >= 4 && lane == 0)
            __hip_atomic_store((u64*)((char*)p.ws + (16u << 20) + 4096) + b * 4 + wave - 4, ((u64)tg << 32) | stp.t[31], __ATOMIC_RELAXED, __HIP_MEMORY_SCOPE_AGENT);
        if (AMP_SEL >= 100) {
            if (b == 255 && wave == 0) {
                const rsrc_t rw = mk(p.ws, 32u << 20);
                u32x4 v; gld_ho(v, (16u << 20) + lane * 16, rw, 0); waitv1<0>(v);
                const u32 t0 = stp.t[0];
                int mn = min(min((int)(v[0] - t0), (int)(v[1] - t0)), min((int)(v[2] - t0), (int)(v[3] - t0)));
                int mx = max(max((int)(v[0] - t0), (int)(v[1] - t0)), max((int)(v[2] - t0), (int)(v[3] - t0)));
                for (int o = 32; o; o >>= 1) { mn = min(mn, __shfl_xor(mn, o)); mx = max(mx, __shfl_xor(mx, o)); }
                mn = __builtin_amdgcn_readfirstlane(mn); mx = __builtin_amdgcn_readfirstlane(mx);
                d = AMP_SEL == 100 ? (u32)((int)(stp.t[31] - t0) - mn) : AMP_SEL == 101 ? (u32)(mx - mn) : 200;
                if (AMP_SEL == 102) {
                    int me = 0;
                    for (int k = 0; k < 8; ++k) {
                        for (u32 spins = 0; spins < 100000; ++spins) {
                            gld_ho(v, (16u << 20) + 4096 + k * 1024 + lane * 16, rw, 0); waitv1<0>(v);
                            if (__all(v[1] == tg && v[3] == tg)) break;
                        }
                        me = max(me, max((int)(v[0] - t0), (int)(v[2] - t0)));
                    }
                    for (int o = 32; o; o >>= 1) me = max(me, __shfl_xor(me, o));
                    me = __builtin_amdgcn_readfirstlane(me);
                    d = me - mn;
                }
                const u32 tgt = t0 + mn + AMP_BASE + d * AMP_K;
                while ((int)((u32)__builtin_amdgcn_s_memrealtime() - tgt) < 0) __builtin_amdgcn_s_sleep(4);
            }
        } else if (AMP_SEL >= 0) {
            if (b == AMP_B && wave == AMP_W) {
                d = stp.t[AMP_SEL < 0 ? 0 : AMP_SEL % 100] - stp.t[0];
                const u32 tgt = stp.t[0] + AMP_BASE + d * AMP_K;
                while ((int)((u32)__builtin_amdgcn_s_memrealtime() - tgt) < 0) __builtin_amdgcn_s_sleep(4);
            }
        }
    }
}

extern "C" void kernel_launch(void* const* d_in, const int* in_sizes, int n_in, void* d_out, int out_size, void* d_ws,
                              size_t ws_size, hipStream_t stream) {
    Params p;
    p.x = d_in[0]; p.qw = d_in[1]; p.sc = d_in[2]; p.ze = d_in[3]; p.U = d_in[4]; p.V = d_in[5];
    p.y = d_out; p.ws = d_ws;
    quip_kernel<<<256, 512, 0, stream>>>(p);
}
```

```cpp
#define PROF 1
#define MROT 3
#define XROT 1
#define AMP_SEL 2
#define AMP_B 160
#define AMP_W 4
#include <hip/hip_runtime.h>
#include <stdint.h>
#pragma clang diagnostic ignored "-Wint-to-pointer-cast"

#define MLA 20
#define XA 2
#define YLA 8
#define YR 6
#define XTH 8

#define CPFW 3
#define CPFN 7
typedef unsigned u32;
typedef unsigned long long u64;
using bf16x8 = __attribute__((ext_vector_type(8))) short;
using s16x4 = __attribute__((ext_vector_type(4))) short;
using f32x4 = __attribute__((ext_vector_type(4))) float;
using u32x4 = __attribute__((ext_vector_type(4))) unsigned;
using u32x3 = __attribute__((ext_vector_type(3))) unsigned;
using u32x2 = __attribute__((ext_vector_type(2))) unsigned;
using bf16x2_t = __attribute__((ext_vector_type(2))) __bf16;
using f32x2 = __attribute__((ext_vector_type(2))) float;
#define LAS __attribute__((address_space(3)))
typedef __amdgpu_buffer_rsrc_t rsrc_t;

constexpr int S_ = 2048, D_ = 640, F_ = 1536;
constexpr u32 XF_OFF = 0, XF_BYTES = S_ * D_ * 2;
constexpr u32 MF_OFF = XF_BYTES, MF_BYTES = 2 * D_ * F_ * 2;
constexpr u32 FL_OFF = MF_OFF + MF_BYTES;
constexpr u32 EP_OFF = FL_OFF + 4096;
constexpr u32 WS_BYTES = EP_OFF + 256;
constexpr int LDS_BYTES = 163840;
constexpr u32 CTL_OFF = 162816;
constexpr u32 TOP_OFF = 129024;
constexpr u64 TAGC = 0x9E3779B97F4A7C15ull;

struct Params {
    const void *x, *qw, *sc, *ze, *U, *V;
    void *y, *ws;
};

__device__ __forceinline__ rsrc_t mk(const void* p, u32 bytes) { return __builtin_amdgcn_make_buffer_rsrc((void*)p, (short)0, (int)bytes, 0x00020000); }
__device__ __forceinline__ void glds(u32 m0v, u32 voff, rsrc_t r, u32 soff) {
    asm volatile("s_mov_b32 m0, %0\n\ts_nop 0\n\tbuffer_load_dwordx4 %1, %2, %3 offen lds" ::"s"(m0v), "v"(voff), "s"(r), "s"(soff) : "memory");
}
__device__ __forceinline__ void gld(u32x4& d, u32 voff, rsrc_t r, u32 soff) {
    asm volatile("buffer_load_dwordx4 %0, %1, %2, %3 offen" : "=v"(d) : "v"(voff), "s"(r), "s"(soff) : "memory");
}
__device__ __forceinline__ void gld_ho(u32x4& d, u32 voff, rsrc_t r, u32 soff) {
    asm volatile("buffer_load_dwordx4 %0, %1, %2, %3 offen sc1" : "=v"(d) : "v"(voff), "s"(r), "s"(soff) : "memory");
}
__device__ __forceinline__ void gld_ho2(u32x2& d, u32 voff, rsrc_t r, u32 soff) {
    asm volatile("buffer_load_dwordx2 %0, %1, %2, %3 offen sc1" : "=v"(d) : "v"(voff), "s"(r), "s"(soff) : "memory");
}
template <int N> __device__ __forceinline__ void waitv() { asm volatile("s_waitcnt vmcnt(%0)" ::"n"(N) : "memory"); }
template <int N, class T> __device__ __forceinline__ void waitv1(T& a) { asm volatile("s_waitcnt vmcnt(%1)" : "+v"(a) : "n"(N) : "memory"); }
template <int N, class T> __device__ __forceinline__ void waitv2(T& a, T& b) { asm volatile("s_waitcnt vmcnt(%2)" : "+v"(a), "+v"(b) : "n"(N) : "memory"); }
__device__ __forceinline__ void barw() { asm volatile("s_waitcnt lgkmcnt(0)\n\ts_barrier" ::: "memory"); }
__device__ __forceinline__ void barn() { asm volatile("s_barrier" ::: "memory"); }
#define SB() __builtin_amdgcn_sched_barrier(0)
__device__ __forceinline__ u32 pk(float a, float b) {
    f32x2 v = {a, b};
    return __builtin_bit_cast(u32, __builtin_convertvector(v, bf16x2_t));
}
__device__ __forceinline__ float fadd(float a, float b) { float d; asm("v_add_f32 %0, %1, %2" : "=v"(d) : "v"(a), "v"(b)); return d; }
__device__ __forceinline__ float ffma(float a, float b, float c) { float d; asm("v_fma_f32 %0, %1, %2, %3" : "=v"(d) : "v"(a), "v"(b), "v"(c)); return d; }
__device__ __forceinline__ bf16x8 ldsr128(u32 a) { return *(const LAS bf16x8*)a; }
__device__ __forceinline__ s16x4 ldstr(u32 a) { return __builtin_amdgcn_ds_read_tr16_b64_v4i16((LAS s16x4*)a); }
__device__ __forceinline__ bf16x8 cat(s16x4 a, s16x4 b) { return __builtin_shufflevector(a, b, 0, 1, 2, 3, 4, 5, 6, 7); }
#define MFMA(a, b, c) __builtin_amdgcn_mfma_f32_16x16x32_bf16(a, b, c, 0, 0, 0)

__shared__ __attribute__((aligned(1024))) char smem[LDS_BYTES];

struct Stamps { u32 t[32]; };
#define STAMP(k) do { u64 t_; asm volatile("s_memrealtime %0\n\ts_waitcnt lgkmcnt(0)" : "=s"(t_) :: "memory"); stp.t[k] = (u32)t_ | 1u; } while (0)
#define PROF_ROLE(b) (((b) >> 3) < 20 ? 0 : ((b) >> 3) < 30 ? 1 : 2)
#define PROF_NB 256

__device__ __forceinline__ u64 ld_epoch(const Params& p) {
    return __hip_atomic_load((const u64*)((const char*)p.ws + EP_OFF), __ATOMIC_RELAXED, __HIP_MEMORY_SCOPE_AGENT);
}
__device__ __forceinline__ void put_ctl(u64 e) {
    const u32 base = (u32)(uintptr_t)smem;
    *(LAS u64*)(base + CTL_OFF) = e + TAGC;
    *(LAS u32*)(base + CTL_OFF + 8) = 0;
    *(LAS u32x2*)(base + CTL_OFF + 16) = u32x2{0, 0};
}
__device__ __forceinline__ u64 get_tag() { return *(const LAS u64*)((u32)(uintptr_t)smem + CTL_OFF); }
__device__ __forceinline__ void publish(const Params& p, int lane, int fidx, u64 tag) {
    waitv<0>();
    if (lane == 0) {
        const u32 old = __hip_atomic_fetch_add((LAS u32*)((u32)(uintptr_t)smem + CTL_OFF + 8), 1u, __ATOMIC_RELAXED, __HIP_MEMORY_SCOPE_WORKGROUP);
        if (old == 3) __hip_atomic_store((u64*)((char*)p.ws + FL_OFF) + fidx, tag, __ATOMIC_RELAXED, __HIP_MEMORY_SCOPE_AGENT);
    }
}
__device__ __forceinline__ void poll(const u64* f, bool active, u64 tag) {
    for (u32 spins = 0; spins < (1u << 16); ++spins) {
        u64 v = tag;
        if (active) v = __hip_atomic_load(f, __ATOMIC_RELAXED, __HIP_MEMORY_SCOPE_AGENT);
        if (__all(v == tag)) return;
        __builtin_amdgcn_s_sleep(1);
    }
}

__device__ __forceinline__ void wait_ready(bool leader, const u64* f, bool active, u64 tag, u32 word) {
    LAS u32* w = (LAS u32*)((u32)(uintptr_t)smem + CTL_OFF + word);
    if (leader) {
        poll(f, active, tag);
        __hip_atomic_store(w, 1u, __ATOMIC_RELAXED, __HIP_MEMORY_SCOPE_WORKGROUP);
    } else {
        for (u32 spins = 0; spins < (1u << 22); ++spins) {
            if (__builtin_amdgcn_readfirstlane(__hip_atomic_load(w, __ATOMIC_RELAXED, __HIP_MEMORY_SCOPE_WORKGROUP))) break;
            __builtin_amdgcn_s_sleep(1);
        }
    }
    asm volatile("" ::: "memory");
}

constexpr u32 mbslot(int st) { return st % 4 == 3 ? TOP_OFF : (st % 4) * 24576; }
constexpr u32 maslot(int st) { return st % 3 == 2 ? TOP_OFF + 24576 : 73728 + (st % 3) * 8192; }
constexpr u32 MS_OFF = 90112, MZ_OFF = MS_OFF + 3072;
static_assert(MLA <= 20, "slot reuse");
constexpr int MAR = 8;
struct MCtx { u32 voffA, voffB[6], wa[4], wb, sca; rsrc_t rq, rv; int rot; };
__device__ __forceinline__ u32 krot(int st, int rot) {
    if (MROT == 0) return st;
    const int k = st + rot;
    return k >= 12 ? k - 12 : k;
}
#define MORD 0
constexpr int mord(int k) {
    constexpr int o0[10] = {0, ~0, 1, ~1, 2, 3, ~2, 4, ~3, 5};
    constexpr int o1[10] = {~0, 0, ~1, 1, ~2, 2, ~3, 3, 4, 5};
    constexpr int o2[10] = {~0, ~1, ~2, ~3, 0, 1, 2, 3, 4, 5};
    return MORD == 0 ? o0[k] : MORD == 1 ? o1[k] : o2[k];
}
template <int P> struct MP {
    static constexpr int ST = P / 10, K = P % 10;
    static constexpr bool isA = mord(K) < 0;
    static constexpr int idx = isA ? ~mord(K) : mord(K);
    static constexpr int ar = (ST * 4 + idx) % MAR;
    static __device__ __forceinline__ void issue(u32x4 (&r)[MAR], const MCtx& c) {
        if constexpr (isA) gld(r[ar], c.voffA, c.rq, krot(ST, c.rot) * 256 + idx * 24576);
        else glds(c.wb + mbslot(ST) + idx * 1024, c.voffB[idx], c.rv, krot(ST, c.rot) * 196608);
    }
    static __device__ __forceinline__ void pro(u32x4 (&r)[MAR], const MCtx& c) {
        issue(r, c);
        if constexpr (P + 1 < MLA) MP<P + 1>::pro(r, c);
    }
    static __device__ __forceinline__ void run(u32x4 (&r)[MAR], const MCtx& c, f32x4& s4, f32x4& z4, Stamps& stp) {
        constexpr int REM = (119 - P) < (MLA - 1) ? (119 - P) : (MLA - 1);
        if constexpr (K == 0) {
            s4 = *(const LAS f32x4*)(c.sca + MS_OFF + krot(ST, c.rot) * 256);
            z4 = *(const LAS f32x4*)(c.sca + MZ_OFF + krot(ST, c.rot) * 256);
        }
        if constexpr (isA) {
            waitv1<REM>(r[ar]);
            const u32x4 q = r[ar];
            u32x2 o;
            o[0] = pk(ffma((float)(int)q[0], s4[0], z4[0]), ffma((float)(int)q[1], s4[1], z4[1]));
            o[1] = pk(ffma((float)(int)q[2], s4[2], z4[2]), ffma((float)(int)q[3], s4[3], z4[3]));
            *(LAS u32x2*)(c.wa[idx] + maslot(ST)) = o;
        } else if constexpr (K == 9) {
            waitv<REM>();
        }
        if constexpr (K == 9) { barw(); STAMP(2 + ST); }
        if constexpr (P + MLA < 120) MP<P + MLA>::issue(r, c);
        if constexpr (P + 1 < 120) MP<P + 1>::run(r, c, s4, z4, stp);
    }
};

struct MSet { bf16x8 a[4]; s16x4 bl[3], bh[3]; };
template <bool DOM, bool DOR>
__device__ __forceinline__ void m_phase(f32x4 (&acc)[4][3], const MSet& m, MSet& r, u32 aa, const u32 (&bb)[3], u32 aoff, u32 boff) {
#pragma unroll
    for (int i = 0; i < 12; ++i) {
        if (DOM) acc[i / 3][i % 3] = MFMA(m.a[i / 3], cat(m.bl[i % 3], m.bh[i % 3]), acc[i / 3][i % 3]);
        if (DOR) {
            if (i == 0) r.bl[0] = ldstr(bb[0] + boff);
            if (i == 1) r.bh[0] = ldstr(bb[0] + boff + 1536);
            if (i == 2) r.a[0] = ldsr128(aa + aoff);
            if (i == 3) r.bl[1] = ldstr(bb[1] + boff);
            if (i == 4) r.bh[1] = ldstr(bb[1] + boff + 1536);
            if (i == 5) r.bl[2] = ldstr(bb[2] + boff);
            if (i == 6) r.bh[2] = ldstr(bb[2] + boff + 1536);
            if (i >= 7 && i < 10) r.a[i - 6] = ldsr128(aa + aoff + (i - 6) * 2048);
        }
        SB();
    }
}

__device__ __forceinline__ void m_prod(const Params& p, int pw, int lane, int et, int gt, int half, Stamps& stp) {
    const u32 base = (u32)(uintptr_t)smem;
    const int q = lane >> 4, c = lane & 15;
    const int kb = half * 768;
    MCtx cx;
    cx.rq = mk(p.qw, D_ * F_ * 4); cx.rv = mk(p.V, F_ * F_ * 2);
    cx.voffA = ((et * 64 + 16 * pw + q) * F_ + kb + 4 * c) * 4;
#pragma unroll
    for (int j = 0; j < 6; ++j) {
        const int P = (6 * pw + j) * 1024 + 16 * lane;
        const int r = P / 384, o = P - r * 384;
        const int sw = ((r >> 1) & 1) | (((r >> 3) & 1) << 1);
        cx.voffB[j] = (kb + r) * (F_ * 2) + gt * 384 + (((o >> 5) ^ sw) << 5) + (o & 16);
    }
#pragma unroll
    for (int i = 0; i < 4; ++i) {
        const int row = 16 * pw + 4 * i + q;
        cx.wa[i] = base + row * 128 + (((c >> 1) ^ ((row >> 1) & 7)) << 4) + (c & 1) * 8;
    }
    cx.wb = base + pw * 6144;
    cx.sca = base + c * 16;
    cx.rot = MROT == 1 ? et % 5 : MROT == 2 ? 2 * (et % 5) : MROT == 3 ? ((et % 5) + 3 * (gt & 3)) % 12 : MROT == 4 ? (gt & 3) : 0;
    u32x4 r[MAR];
    MP<0>::pro(r, cx);
    STAMP(1);
    barn();
    f32x4 s4, z4;
    MP<0>::run(r, cx, s4, z4, stp);
}

__device__ __forceinline__ void m_cons(const Params& p, int wave, int lane, int et, int gt, int half, Stamps& stp) {
    const u32 base = (u32)(uintptr_t)smem;
    const int q = lane >> 4, c = lane & 15;
    const int kb = half * 768;
    {
        const int t = wave * 64 + lane;
        if (t < 192) {
            const u32x2 sv = *(const u32x2*)((const char*)p.sc + (kb + 4 * t) * 2);
            const u32x2 zv = *(const u32x2*)((const char*)p.ze + (kb + 4 * t) * 2);
            f32x4 s4, z4;
            s4[0] = __uint_as_float(sv[0] << 16); s4[1] = __uint_as_float(sv[0] & 0xffff0000u);
            s4[2] = __uint_as_float(sv[1] << 16); s4[3] = __uint_as_float(sv[1] & 0xffff0000u);
            z4[0] = -__uint_as_float(zv[0] << 16) * s4[0]; z4[1] = -__uint_as_float(zv[0] & 0xffff0000u) * s4[1];
            z4[2] = -__uint_as_float(zv[1] << 16) * s4[2]; z4[3] = -__uint_as_float(zv[1] & 0xffff0000u) * s4[3];
            *(LAS f32x4*)(base + MS_OFF + t * 16) = s4;
            *(LAS f32x4*)(base + MZ_OFF + t * 16) = z4;
        } else if (t == 192) {
            put_ctl(ld_epoch(p));
        }
    }
    barw();
    STAMP(1);
    const int qq = c >> 2, pp = c & 3;
    const int sw = (qq >> 1) | ((q & 1) << 1);
    u32 ab[2], bb[3];
    ab[0] = base + c * 128 + ((q ^ ((c >> 1) & 7)) << 4);
    ab[1] = ab[0] ^ 64;
#pragma unroll
    for (int n = 0; n < 3; ++n) bb[n] = base + (8 * q + qq) * 384 + (((3 * wave + n) ^ sw) << 5) + 8 * pp;
    f32x4 acc[4][3] = {};
    MSet s0, s1;
#pragma unroll
    for (int st = 0; st < 12; ++st) {
        SB(); barn(); SB();
        if (st == 0) m_phase<false, true>(acc, s1, s0, ab[0], bb, maslot(st), mbslot(st));
        else m_phase<true, true>(acc, s1, s0, ab[0], bb, maslot(st), mbslot(st));
        m_phase<true, true>(acc, s0, s1, ab[1], bb, maslot(st), mbslot(st) + 12288);
    }
    m_phase<true, false>(acc, s1, s0, 0, bb, 0, 0);
    const rsrc_t rw = mk(p.ws, WS_BYTES);
#pragma unroll
    for (int ebl = 0; ebl < 2; ++ebl)
#pragma unroll
        for (int n = 0; n < 3; ++n) {
            const int nt = 3 * wave + n;
            const int gb = 2 * gt + nt / 6, np = nt % 6;
            u32x4 v;
            v[0] = pk(acc[2 * ebl][n][0], acc[2 * ebl][n][1]);
            v[1] = pk(acc[2 * ebl][n][2], acc[2 * ebl][n][3]);
            v[2] = pk(acc[2 * ebl + 1][n][0], acc[2 * ebl + 1][n][1]);
            v[3] = pk(acc[2 * ebl + 1][n][2], acc[2 * ebl + 1][n][3]);
            const u32 off = MF_OFF + (((half * 16 + gb) * 20 + 2 * et + ebl) * 6 + np) * 1024;
            __builtin_amdgcn_raw_buffer_store_b128(v, rw, lane * 16, off, 16);
        }
}

constexpr u32 xslot(int st) { return st % 4 == 1 ? TOP_OFF : st % 4 == 0 ? 0 : (st % 4 - 1) * 32768; }
struct XCtx { u32 vU[2], vX[2], ldsb; rsrc_t ru, rx; int rot; };
template <int ST> struct XP {
    static __device__ __forceinline__ void issue(const XCtx& c) {
        int k = ST;
        if (XROT) { k = ST + c.rot; k = k >= 10 ? k - 10 : k; }
#pragma unroll
        for (int j = 0; j < 4; ++j) {
            glds(c.ldsb + xslot(ST) + 16384 + j * 1024, c.vX[j & 1], c.rx, k * 128 + j * 8 * 1280);
            glds(c.ldsb + xslot(ST) + j * 1024, c.vU[j >> 1], c.ru, k * 64 * 1280 + j * 4 * 1280);
        }
    }
    static __device__ __forceinline__ void pro(const XCtx& c) {
        issue(c);
        if constexpr (ST + 1 < XA) XP<ST + 1>::pro(c);
    }
    static __device__ __forceinline__ void run(const XCtx& c, Stamps& stp) {
        constexpr int REM = (9 - ST) < (XA - 1) ? (9 - ST) : (XA - 1);
        waitv<REM * 8>();
        barn();
        STAMP(2 + ST);
        if constexpr (ST + XA < 10) XP<ST + XA>::issue(c);
        if constexpr (ST + 1 < 10) XP<ST + 1>::run(c, stp);
    }
};
static_assert(XA <= 2, "slot reuse");

struct XSet { s16x4 al[4], ah[4]; bf16x8 b[4]; };
template <bool DOM, bool DOR>
__device__ __forceinline__ void x_phase(f32x4 (&acc)[4][4], const XSet& m, XSet& r, const u32 (&ab)[4], u32 bb, u32 aoff, u32 boff) {
#pragma unroll
    for (int i = 0; i < 16; ++i) {
        if (DOM) acc[i / 4][i % 4] = MFMA(cat(m.al[i / 4], m.ah[i / 4]), m.b[i % 4], acc[i / 4][i % 4]);
        if (DOR) {
            if (i == 0) r.al[0] = ldstr(ab[0] + aoff);
            if (i == 1) r.ah[0] = ldstr(ab[0] + aoff + 1024);
            if (i >= 2 && i < 6) r.b[i - 2] = ldsr128(bb + boff + (i - 2) * 2048);
            if (i >= 6 && i < 12 && !(i & 1)) r.al[(i - 4) / 2] = ldstr(ab[(i - 4) / 2] + aoff);
            if (i >= 6 && i < 12 && (i & 1)) r.ah[(i - 4) / 2] = ldstr(ab[(i - 4) / 2] + aoff + 1024);
        }
        SB();
    }
}

__device__ __forceinline__ void x_prod(const Params& p, int pw, int lane, int sb, int eblk, Stamps& stp) {
    const u32 base = (u32)(uintptr_t)smem;
    const int q = lane >> 4, c = lane & 15;
    XCtx cx;
    cx.ru = mk(p.U, D_ * D_ * 2); cx.rx = mk(p.x, S_ * D_ * 2);
#pragma unroll
    for (int h = 0; h < 2; ++h) {
        cx.vU[h] = (16 * pw + q) * 1280 + eblk * 256 + (((c >> 1) ^ (q | (h << 2))) << 5) + (c & 1) * 16;
        cx.vX[h] = (sb * 128 + 32 * pw + (lane >> 3)) * 1280 + (((lane & 7) ^ ((4 * h + q) & 7)) << 4);
    }
    cx.ldsb = base + pw * 4096;
    cx.rot = XROT == 1 ? (2 * eblk + 5 * (sb & 1)) % 10 : XROT == 2 ? 2 * eblk : (2 * eblk + (sb & 1)) % 10;
    XP<0>::pro(cx);
    STAMP(1);
    XP<0>::run(cx, stp);
}

__device__ __forceinline__ void x_cons(const Params& p, int wave, int lane, int sb, int eblk, Stamps& stp) {
    const u32 base = (u32)(uintptr_t)smem;
    const int q = lane >> 4, c = lane & 15;
    const int we = wave & 1, ws = wave >> 1;
    const int qq = c >> 2, pp = c & 3;
    const int sw = qq | ((q & 1) << 2);
    u64 ep = 0;
    if (wave == 0 && lane == 0) ep = ld_epoch(p);
    u32 ab[4], bb[2];
#pragma unroll
    for (int t = 0; t < 4; ++t) ab[t] = base + (8 * q + qq) * 256 + (((4 * we + t) ^ sw) << 5) + 8 * pp;
    bb[0] = base + 16384 + (64 * ws + c) * 128 + ((q ^ ((c >> 1) & 7)) << 4);
    bb[1] = bb[0] ^ 64;
    f32x4 acc[4][4] = {};
    XSet s0, s1;
#pragma unroll
    for (int st = 0; st < 10; ++st) {
        if (st == 2 && wave == 0 && lane == 0) put_ctl(ep);
        SB(); barn(); SB();
        if (st == 0) x_phase<false, true>(acc, s1, s0, ab, bb[0], xslot(st), xslot(st));
        else x_phase<true, true>(acc, s1, s0, ab, bb[0], xslot(st), xslot(st));
        x_phase<true, true>(acc, s0, s1, ab, bb[1], xslot(st) + 8192, xslot(st));
    }
    x_phase<true, false>(acc, s1, s0, ab, 0, 0, 0);
    const rsrc_t rw = mk(p.ws, WS_BYTES);
#pragma unroll
    for (int ebl = 0; ebl < 2; ++ebl)
#pragma unroll
        for (int u = 0; u < 4; ++u) {
            u32x4 v;
            v[0] = pk(acc[2 * ebl][u][0], acc[2 * ebl][u][1]);
            v[1] = pk(acc[2 * ebl][u][2], acc[2 * ebl][u][3]);
            v[2] = pk(acc[2 * ebl + 1][u][0], acc[2 * ebl + 1][u][1]);
            v[3] = pk(acc[2 * ebl + 1][u][2], acc[2 * ebl + 1][u][3]);
            const u32 off = XF_OFF + ((sb * 20 + 4 * eblk + 2 * we + ebl) * 8 + 4 * ws + u) * 1024;
            __builtin_amdgcn_raw_buffer_store_b128(v, rw, lane * 16, off, 16);
        }
}

constexpr u32 YQ = 1552, YEB = 4 * YQ;
__device__ __forceinline__ u32 add2(u32 a, u32 b) {
    const float lo = fadd(__uint_as_float(a << 16), __uint_as_float(b << 16));
    const float hi = fadd(__uint_as_float(a & 0xffff0000u), __uint_as_float(b & 0xffff0000u));
    return pk(lo, hi);
}
struct YCtx { u32 voff, wa[3]; rsrc_t rw; };
template <int P> struct YP {
    static constexpr int ST = P / 3, J = P % 3;
    static __device__ __forceinline__ void issue(u32x4 (&r)[YLA][2], const YCtx& c) {
        gld_ho(r[P % YLA][0], c.voff, c.rw, (ST * 12 + J) * 1024);
        gld_ho(r[P % YLA][1], c.voff, c.rw, (ST * 12 + J) * 1024 + 16 * 120 * 1024);
    }
    static __device__ __forceinline__ void pro(u32x4 (&r)[YLA][2], const YCtx& c) {
        issue(r, c);
        if constexpr (P + 1 < YLA) YP<P + 1>::pro(r, c);
    }
    static __device__ __forceinline__ void run(u32x4 (&r)[YLA][2], const YCtx& c, Stamps& stp) {
        constexpr int REM = (29 - P) < (YLA - 1) ? (29 - P) : (YLA - 1);
        waitv2<REM * 2>(r[P % YLA][0], r[P % YLA][1]);
        u32x4 o;
#pragma unroll
        for (int e = 0; e < 4; ++e) o[e] = add2(r[P % YLA][0][e], r[P % YLA][1][e]);
        *(LAS u32x4*)(c.wa[J] + ST * 2 * YEB) = o;
        if constexpr (P + YLA < 30) YP<P + YLA>::issue(r, c);
        if constexpr (J == 2) { barw(); STAMP(20 + ST); }
        if constexpr (P + 1 < 30) YP<P + 1>::run(r, c, stp);
    }
};

__device__ __forceinline__ void y_load(const Params& p, int pw, int lane, int gb, u64 tag, bool updater, Stamps& stp) {
    const u32 base = (u32)(uintptr_t)smem;
    const int q = lane >> 4, c = lane & 15;
    YCtx cx;
    cx.rw = mk(p.ws, WS_BYTES);
    cx.voff = MF_OFF + (gb * 120 + 3 * pw) * 1024 + lane * 16;
#pragma unroll
    for (int j = 0; j < 3; ++j) {
        const int ci = 3 * pw + j;
        cx.wa[j] = base + (ci / 6) * YEB + q * YQ + (16 * (ci % 6) + c) * 16;
    }
    const u64* fl = (const u64*)((const char*)p.ws + FL_OFF);
    wait_ready(pw == 0, fl + (gb >> 1) * 20 + lane, lane < 20, tag, 16);
    STAMP(19);
    u32x4 r[YLA][2];
    YP<0>::pro(r, cx);
    YP<0>::run(r, cx, stp);
    if (updater && pw == 0) {
#pragma unroll 1
        for (u32 spins = 0; spins < (1u << 16); ++spins) {
            u32x4 f0, f1;
            gld_ho(f0, FL_OFF + lane * 32, cx.rw, 0);
            gld_ho(f1, FL_OFF + lane * 32 + 16, cx.rw, 0);
            waitv2<0>(f0, f1);
            const u32 tl = (u32)tag, th = (u32)(tag >> 32);
            const bool ok = f0[0] == tl && f0[1] == th && f0[2] == tl && f0[3] == th && f1[0] == tl && f1[1] == th && f1[2] == tl && f1[3] == th;
            if (__all(ok)) break;
            __builtin_amdgcn_s_sleep(1);
        }
        if (lane == 0) __hip_atomic_store((u64*)((char*)p.ws + EP_OFF), tag, __ATOMIC_RELAXED, __HIP_MEMORY_SCOPE_AGENT);
    }
}

__device__ __forceinline__ void y_mma(const Params& p, int w, int lane, int sb, int gb, u64 tag, Stamps& stp) {
    const u32 base = (u32)(uintptr_t)smem;
    const int q = lane >> 4, c = lane & 15;
    const rsrc_t rw = mk(p.ws, WS_BYTES);
    const u64* fl = (const u64*)((const char*)p.ws + FL_OFF);
    wait_ready(w == 0, fl + 160 + sb * 5 + lane, lane < 5, tag, 20);
    STAMP(18);
    bf16x8 xa[20][2];
#pragma unroll
    for (int eb = 0; eb < 20; ++eb) {
#pragma unroll
        for (int u = 0; u < 2; ++u)
            xa[eb][u] = __builtin_bit_cast(bf16x8, __builtin_amdgcn_raw_buffer_load_b128(rw, XF_OFF + (sb * 160 + 2 * w) * 1024 + lane * 16, (eb * 8 + u) * 1024, 16));
        if (2 * eb + 2 > XTH) waitv<XTH>();
    }
    STAMP(19);
    const u32 bb = base + q * YQ + c * 96;
    f32x4 acc[2][6] = {};
    bf16x8 b[YR];
    SB(); barn(); SB();
#pragma unroll
    for (int f = 0; f < YR; ++f) b[f] = ldsr128(bb + (f / 6) * YEB + (f % 6) * 16);
    SB();
#pragma unroll
    for (int f = 0; f < 120; ++f) {
        const int eb = f / 6, n = f % 6;
        acc[0][n] = MFMA(xa[eb][0], b[f % YR], acc[0][n]);
        acc[1][n] = MFMA(xa[eb][1], b[f % YR], acc[1][n]);
        SB();
        if (f + YR < 120) {
            if ((f + YR) % 12 == 0) { barn(); SB(); }
            b[f % YR] = ldsr128(bb + ((f + YR) / 6) * YEB + ((f + YR) % 6) * 16);
            SB();
        }
    }
    STAMP(30);
    const rsrc_t ry = mk(p.y, S_ * F_ * 2);
    const u32 yo = ((sb * 128 + 32 * w + 4 * q) * F_ + gb * 96 + 6 * c) * 2;
#pragma unroll
    for (int u = 0; u < 2; ++u)
#pragma unroll
        for (int r = 0; r < 4; ++r) {
            u32x3 v;
            v[0] = pk(acc[u][0][r], acc[u][1][r]);
            v[1] = pk(acc[u][2][r], acc[u][3][r]);
            v[2] = pk(acc[u][4][r], acc[u][5][r]);
            __builtin_amdgcn_raw_buffer_store_b96(v, ry, yo, (16 * u + r) * F_ * 2, 2);
        }
    asm volatile(".Lqk_end:");
}

__global__ __launch_bounds__(512, 2) void quip_kernel(Params p) {
    const int b = blockIdx.x, xcd = b & 7, i = b >> 3;
    const int lane = threadIdx.x & 63;
    const int wave = __builtin_amdgcn_readfirstlane(threadIdx.x >> 6);
    Stamps stp;
    u32 cpf = 0;
    if (wave == CPFW) {
        u64 pc; u32 len;
        asm volatile("s_getpc_b64 %0\n.Lqk_start:\n\ts_mov_b32 %1, .Lqk_end-.Lqk_start" : "=s"(pc), "=s"(len));
        const rsrc_t rc = mk((const void*)pc, len);
        asm volatile("buffer_load_dword %0, %1, %2, 0 offen" : "=v"(cpf) : "v"(((i % CPFN) * 64 + lane) * 128), "s"(rc) : "memory");
    }
#pragma unroll
    for (int k = 0; k < 32; ++k) stp.t[k] = 0;
    STAMP(0);
    u64 c0_ = __builtin_amdgcn_s_memtime();
    const int sb2 = 8 * (xcd & 1) + (i & 7), gb2 = 4 * (xcd >> 1) + (i >> 3);
    const int half = xcd & 1, et = 5 * ((xcd >> 1) & 1) + i % 5, gt = 4 * (xcd >> 2) + i / 5;
    const int j = i - 20, sb = 2 * xcd + j / 5, eblk = j % 5;
    if (wave >= 4) {
        if (i < 20) m_prod(p, wave - 4, lane, et, gt, half, stp);
        else if (i < 30) x_prod(p, wave - 4, lane, sb, eblk, stp);
        else barn();
        STAMP(15);
        y_mma(p, wave - 4, lane, sb2, gb2, get_tag(), stp);
    } else {
        int fidx;
        if (i < 20) { m_cons(p, wave, lane, et, gt, half, stp); fidx = gt * 20 + et * 2 + half; }
        else if (i < 30) { x_cons(p, wave, lane, sb, eblk, stp); fidx = 160 + sb * 5 + eblk; }
        else {
            if (wave == 0 && lane == 0) put_ctl(ld_epoch(p));
            barw();
            fidx = b;
        }
        STAMP(15);
        const u64 tag = get_tag();
        publish(p, lane, fidx, tag);
        asm volatile("" :: "v"(cpf));
        STAMP(17);
        y_load(p, wave, lane, gb2, tag, b == 255, stp);
    }
    waitv<0>();
    STAMP(31);
    if (b == AMP_B && wave == AMP_W) {
        const u32 d = stp.t[AMP_SEL] - stp.t[0];
        const u32 tgt = stp.t[0] + 4000 + d * 5;
        while ((int)((u32)__builtin_amdgcn_s_memrealtime() - tgt) < 0) __builtin_amdgcn_s_sleep(4);
    }
}

extern "C" void kernel_launch(void* const* d_in, const int* in_sizes, int n_in, void* d_out, int out_size, void* d_ws,
                              size_t ws_size, hipStream_t stream) {
    Params p;
    p.x = d_in[0]; p.qw = d_in[1]; p.sc = d_in[2]; p.ze = d_in[3]; p.U = d_in[4]; p.V = d_in[5];
    p.y = d_out; p.ws = d_ws;
    quip_kernel<<<256, 512, 0, stream>>>(p);
}
```

```cpp
#include <hip/hip_runtime.h>
#include <stdint.h>
#pragma clang diagnostic ignored "-Wint-to-pointer-cast"

#define MLA 32
#define XA 3
#define YLA 8
#define YR 6
#define XTH 8

#define YAUX 2
#define SND 1
#define XPG 0
#define XTD 2
#define YH 6
#define CPFW 3
#define CPFN 7
typedef unsigned u32;
typedef unsigned long long u64;
using bf16x8 = __attribute__((ext_vector_type(8))) short;
using s16x4 = __attribute__((ext_vector_type(4))) short;
using f32x4 = __attribute__((ext_vector_type(4))) float;
using u32x4 = __attribute__((ext_vector_type(4))) unsigned;
using u32x3 = __attribute__((ext_vector_type(3))) unsigned;
using u32x2 = __attribute__((ext_vector_type(2))) unsigned;
using bf16x2_t = __attribute__((ext_vector_type(2))) __bf16;
using f32x2 = __attribute__((ext_vector_type(2))) float;
#define LAS __attribute__((address_space(3)))
typedef __amdgpu_buffer_rsrc_t rsrc_t;

constexpr int S_ = 2048, D_ = 640, F_ = 1536;
constexpr u32 XF_OFF = 0, XF_BYTES = S_ * D_ * 2;
constexpr u32 MF_OFF = XF_BYTES, MF_BYTES = D_ * F_ * 2;
constexpr u32 EX_OFF = MF_OFF + MF_BYTES, EX_BYTES = 160 * 24576;
constexpr u32 FL_OFF = EX_OFF + EX_BYTES;
#define FLS 16
#define PSL 4
constexpr u32 EP_OFF = FL_OFF + 65536;
constexpr u32 WS_BYTES = EP_OFF + 256;
constexpr int LDS_BYTES = 163840;
constexpr u32 CTL_OFF = 162816;
constexpr u32 TOP_OFF = 129024;
constexpr u64 TAGC = 0x9E3779B97F4A7C15ull;

struct Params {
    const void *x, *qw, *sc, *ze, *U, *V;
    void *y, *ws;
};

__device__ __forceinline__ rsrc_t mk(const void* p, u32 bytes) { return __builtin_amdgcn_make_buffer_rsrc((void*)p, (short)0, (int)bytes, 0x00020000); }
__device__ __forceinline__ void glds(u32 m0v, u32 voff, rsrc_t r, u32 soff) {
    asm volatile("s_mov_b32 m0, %0\n\ts_nop 0\n\tbuffer_load_dwordx4 %1, %2, %3 offen lds" ::"s"(m0v), "v"(voff), "s"(r), "s"(soff) : "memory");
}
__device__ __forceinline__ void gld(u32x4& d, u32 voff, rsrc_t r, u32 soff) {
    asm volatile("buffer_load_dwordx4 %0, %1, %2, %3 offen" : "=v"(d) : "v"(voff), "s"(r), "s"(soff) : "memory");
}
__device__ __forceinline__ void gld_ho(u32x4& d, u32 voff, rsrc_t r, u32 soff) {
    asm volatile("buffer_load_dwordx4 %0, %1, %2, %3 offen sc1" : "=v"(d) : "v"(voff), "s"(r), "s"(soff) : "memory");
}
__device__ __forceinline__ void gld_ho2(u32x2& d, u32 voff, rsrc_t r, u32 soff) {
    asm volatile("buffer_load_dwordx2 %0, %1, %2, %3 offen sc1" : "=v"(d) : "v"(voff), "s"(r), "s"(soff) : "memory");
}
template <int N> __device__ __forceinline__ void waitv() { asm volatile("s_waitcnt vmcnt(%0)" ::"n"(N) : "memory"); }
template <int N, class T> __device__ __forceinline__ void waitv1(T& a) { asm volatile("s_waitcnt vmcnt(%1)" : "+v"(a) : "n"(N) : "memory"); }
template <int N, class T> __device__ __forceinline__ void waitv2(T& a, T& b) { asm volatile("s_waitcnt vmcnt(%2)" : "+v"(a), "+v"(b) : "n"(N) : "memory"); }
__device__ __forceinline__ void barw() { asm volatile("s_waitcnt lgkmcnt(0)\n\ts_barrier" ::: "memory"); }
__device__ __forceinline__ void barn() { asm volatile("s_barrier" ::: "memory"); }
#define SB() __builtin_amdgcn_sched_barrier(0)
__device__ __forceinline__ u32 pk(float a, float b) {
    f32x2 v = {a, b};
    return __builtin_bit_cast(u32, __builtin_convertvector(v, bf16x2_t));
}
__device__ __forceinline__ float fadd(float a, float b) { float d; asm("v_add_f32 %0, %1, %2" : "=v"(d) : "v"(a), "v"(b)); return d; }
__device__ __forceinline__ float ffma(float a, float b, float c) { float d; asm("v_fma_f32 %0, %1, %2, %3" : "=v"(d) : "v"(a), "v"(b), "v"(c)); return d; }
__device__ __forceinline__ bf16x8 ldsr128(u32 a) { return *(const LAS bf16x8*)a; }
__device__ __forceinline__ s16x4 ldstr(u32 a) { return __builtin_amdgcn_ds_read_tr16_b64_v4i16((LAS s16x4*)a); }
__device__ __forceinline__ bf16x8 cat(s16x4 a, s16x4 b) { return __builtin_shufflevector(a, b, 0, 1, 2, 3, 4, 5, 6, 7); }
#define MFMA(a, b, c) __builtin_amdgcn_mfma_f32_16x16x32_bf16(a, b, c, 0, 0, 0)

__shared__ __attribute__((aligned(1024))) char smem[LDS_BYTES];

struct Stamps {};
#define STAMP(k) do {} while (0)

__device__ __forceinline__ u64 ld_epoch(const Params& p) {
    return __hip_atomic_load((const u64*)((const char*)p.ws + EP_OFF), __ATOMIC_RELAXED, __HIP_MEMORY_SCOPE_AGENT);
}
__device__ __forceinline__ void put_ctl(u64 e) {
    const u32 base = (u32)(uintptr_t)smem;
    *(LAS u64*)(base + CTL_OFF) = e + TAGC;
    *(LAS u32*)(base + CTL_OFF + 8) = 0;
    *(LAS u32x2*)(base + CTL_OFF + 16) = u32x2{0, 0};
    *(LAS u32x4*)(base + CTL_OFF + 32) = u32x4{0, 0, 0, 0};
}
__device__ __forceinline__ u64 get_tag() { return *(const LAS u64*)((u32)(uintptr_t)smem + CTL_OFF); }
__device__ __forceinline__ void publish(const Params& p, int lane, int fidx, u64 tag) {
    waitv<0>();
    if (lane == 0) {
        const u32 old = __hip_atomic_fetch_add((LAS u32*)((u32)(uintptr_t)smem + CTL_OFF + 8), 1u, __ATOMIC_RELAXED, __HIP_MEMORY_SCOPE_WORKGROUP);
        if (old == 3) __hip_atomic_store((u64*)((char*)p.ws + FL_OFF) + fidx * FLS, tag, __ATOMIC_RELAXED, __HIP_MEMORY_SCOPE_AGENT);
    }
}
__device__ __forceinline__ void poll(const u64* f, bool active, u64 tag) {
    for (u32 spins = 0; spins < (1u << 16); ++spins) {
        u64 v = tag;
        if (active) v = __hip_atomic_load(f, __ATOMIC_RELAXED, __HIP_MEMORY_SCOPE_AGENT);
        if (__all(v == tag)) return;
        __builtin_amdgcn_s_sleep(PSL);
    }
}

__device__ __forceinline__ void wait_ready(bool leader, const u64* f, bool active, u64 tag, u32 word) {
    LAS u32* w = (LAS u32*)((u32)(uintptr_t)smem + CTL_OFF + word);
    if (leader) {
        poll(f, active, tag);
        __hip_atomic_store(w, 1u, __ATOMIC_RELAXED, __HIP_MEMORY_SCOPE_WORKGROUP);
    } else {
        for (u32 spins = 0; spins < (1u << 22); ++spins) {
            if (__builtin_amdgcn_readfirstlane(__hip_atomic_load(w, __ATOMIC_RELAXED, __HIP_MEMORY_SCOPE_WORKGROUP))) break;
            __builtin_amdgcn_s_sleep(1);
        }
    }
    asm volatile("" ::: "memory");
}

constexpr u32 MS_OFF = 155648, MZ_OFF = MS_OFF + 3072;
constexpr int mb1unit(int st, int h) { return h == 0 ? (st <= 2 ? 2 + st : st <= 7 ? 2 + st : st == 8 ? 11 : 13 + 2 * (st - 9)) : (st <= 6 ? 12 + st : st == 7 ? 10 : 12 + 2 * (st - 8)); }
constexpr int mb1dep(int st, int h) { return st < 8 || (st == 8 && h == 0) ? -1 : 2 * (st - 8) + h; }
constexpr u32 mb1slot(int st, int h) { return mb1unit(st, h) * 8192; }
constexpr u32 mb2slot(int st) { return (st < 7 ? st : 5 + st) * 8192; }
constexpr bool mkeep(int st) { return st < 7 || st == 11; }
#define MROT 6
#define MAR 12
#define XLD 3
#define PEARLY 0
#define MBP 0
#define MBL 2
struct MCtx { u32 voffA, voffC[2], wa[4], wc, sca; rsrc_t rq, rv; int rot; };
__device__ __forceinline__ u32 krot(int st, int rot) {
    if (MROT == 0) return st;
    const int k = st + rot;
    return k >= 12 ? k - 12 : k;
}
__device__ __forceinline__ int mrot(int et, int gt) {
    return MROT == 1 ? et % 5 : MROT == 2 ? 2 * (et % 5) : MROT == 3 ? ((et % 5) + 3 * (gt & 3)) % 12 : MROT == 4 ? (gt & 3) : MROT == 5 ? ((12 * (et % 5) + 2) / 5 + 3 * (gt & 3)) % 12 : MROT == 6 ? (2 * (et % 5) + 3 * (gt & 3)) % 12 : MROT == 7 ? (2 * (et % 5) + (gt & 3)) % 12 : (((MROT >> (4 * (et % 5))) & 15) + 3 * (gt & 3)) % 12;
}
struct MReg { u32x4 a[MAR]; f32x4 s[2], z[2]; };
static_assert(MAR <= 12, "issue order");
constexpr int m_cdone(int P) { return P <= 36 ? 0 : P <= 46 ? 2 * (P - 36) : 20; }
__device__ __forceinline__ void m_cdma(const MCtx& c, int pc) {
    glds(c.wc + mb2slot(pc >> 1) + (pc & 1) * 1024, c.voffC[pc & 1], c.rv, krot(pc >> 1, c.rot) * 196608);
}
template <int P> struct MP {
    static __device__ __forceinline__ void issue(MReg& r, const MCtx& c) {
        gld(r.a[P % MAR], c.voffA, c.rq, krot(P / 4, c.rot) * 256 + (P % 4) * 24576);
    }
    static __device__ __forceinline__ void pro(MReg& r, const MCtx& c) {
        issue(r, c);
        if constexpr (P + 1 < MAR) MP<P + 1>::pro(r, c);
    }
    static __device__ __forceinline__ void run(MReg& r, const MCtx& c, Stamps& stp) {
        constexpr int REM = (47 - P < MAR - 1 ? 47 - P : MAR - 1) + m_cdone(P) - (P >= MAR ? m_cdone(P - MAR) : 0);
        constexpr int ST = P / 4, idx = P % 4;
        if constexpr (P == 0) {
            r.s[0] = *(const LAS f32x4*)(c.sca + MS_OFF + krot(0, c.rot) * 256);
            r.z[0] = *(const LAS f32x4*)(c.sca + MZ_OFF + krot(0, c.rot) * 256);
        }
        if constexpr (idx == 0 && ST < 11) {
            r.s[(ST + 1) & 1] = *(const LAS f32x4*)(c.sca + MS_OFF + krot(ST + 1, c.rot) * 256);
            r.z[(ST + 1) & 1] = *(const LAS f32x4*)(c.sca + MZ_OFF + krot(ST + 1, c.rot) * 256);
        }
        waitv1<REM>(r.a[P % MAR]);
        const u32x4 q = r.a[P % MAR];
        const f32x4 s4 = r.s[ST & 1], z4 = r.z[ST & 1];
        u32x2 o;
        o[0] = pk(ffma((float)(int)q[0], s4[0], z4[0]), ffma((float)(int)q[1], s4[1], z4[1]));
        o[1] = pk(ffma((float)(int)q[2], s4[2], z4[2]), ffma((float)(int)q[3], s4[3], z4[3]));
        *(LAS u32x2*)(c.wa[idx] + ST * 8192) = o;
        if constexpr (P + MAR < 48) MP<P + MAR>::issue(r, c);
#pragma unroll
        for (int k = m_cdone(P); k < m_cdone(P + 1); ++k) m_cdma(c, k);
        if constexpr (idx == 3) { barw(); STAMP(2 + ST); }
        if constexpr (P + 1 < 48) MP<P + 1>::run(r, c, stp);
    }
};
__device__ __forceinline__ void arrive(int lane, u32 word) {
    if (lane == 0) __hip_atomic_fetch_add((LAS u32*)((u32)(uintptr_t)smem + CTL_OFF + word), 1u, __ATOMIC_RELAXED, __HIP_MEMORY_SCOPE_WORKGROUP);
}
__device__ __forceinline__ void await4(u32 word) {
    LAS u32* w = (LAS u32*)((u32)(uintptr_t)smem + CTL_OFF + word);
#pragma unroll 1
    for (u32 spins = 0; spins < (1u << 22); ++spins)
        if (__builtin_amdgcn_readfirstlane(__hip_atomic_load(w, __ATOMIC_RELAXED, __HIP_MEMORY_SCOPE_WORKGROUP)) >= 4) break;
    asm volatile("" ::: "memory");
}

__device__ __forceinline__ void m_prod(const Params& p, int pw, int lane, int et, int gt, int half, Stamps& stp) {
    const u32 base = (u32)(uintptr_t)smem;
    const int q = lane >> 4, c = lane & 15;
    const int kb = half * 768, sg = half ^ (et & 1);
    MCtx cx;
    cx.rq = mk(p.qw, D_ * F_ * 4); cx.rv = mk(p.V, F_ * F_ * 2);
    cx.voffA = ((et * 64 + 16 * pw + q) * F_ + kb + 4 * c) * 4;
    cx.rot = mrot(et, gt);
    MReg r;
    MP<0>::pro(r, cx);
#pragma unroll
    for (int i = 0; i < 4; ++i) {
        const int row = 16 * pw + 4 * i + q;
        cx.wa[i] = base + row * 128 + (((c >> 1) ^ ((row >> 1) & 7)) << 4) + (c & 1) * 8;
    }
#pragma unroll
    for (int h = 0; h < 2; ++h)
        cx.voffC[h] = (kb + 16 * pw + 8 * h + (lane >> 3)) * (F_ * 2) + gt * 384 + (sg ? 256 : 0) + ((((lane >> 1) & 3) ^ ((q & 1) | (h << 1))) << 5) + (lane & 1) * 16;
    cx.wc = base + pw * 2048;
    cx.sca = base + c * 16;
    STAMP(1);
    barn();
    MP<0>::run(r, cx, stp);
#pragma unroll
    for (int k = 20; k < 24; ++k) m_cdma(cx, k);
    waitv<16>(); arrive(lane, 32);
    waitv<8>(); arrive(lane, 36);
    waitv<4>(); arrive(lane, 40);
    waitv<0>(); arrive(lane, 44);
}

struct MS1 { s16x4 bl[4], bh[4]; };
struct MS2 { s16x4 bl[4][2], bh[4][2]; };
typedef bf16x8 MAF[12][2][2];
struct MBC { u32 voff, wb; rsrc_t rv; int rot; };
template <int ST> __device__ __forceinline__ void m_bdma(const MBC& c, int idx) {
    glds(c.wb + mb1slot(ST, idx >> 1) + (idx & 1) * 4096, c.voff, c.rv, krot(ST, c.rot) * 196608 + idx * 16 * 3072);
}
__device__ __forceinline__ u32 adr(u32 b, u32 off) {
    if (off < 65536) return b + off;
    u32 d;
    asm volatile("v_add_u32 %0, %1, %2" : "=v"(d) : "s"(off & ~0xffffu), "v"(b));
    return d + (off & 0xffffu);
}
template <int ST> __device__ __forceinline__ void m_read1(MAF& a, MS1& r, int hh, int j, const u32 (&ab)[2], const u32 (&b1)[4]) {
    if (j == 1) {
        const u32 t = adr(ab[hh], ST * 8192);
        a[ST][hh][0] = ldsr128(t);
        a[ST][hh][1] = ldsr128(t + 2048);
    } else {
        const int n = j ? j - 1 : 0;
        const u32 t = adr(b1[n], mb1slot(ST, hh));
        r.bl[n] = ldstr(t);
        r.bh[n] = ldstr(t + 1024);
    }
}
__device__ __forceinline__ void m_mma1(f32x4 (&acc)[2][6], const bf16x8 (&a)[2], const MS1& m, int i) {
    acc[i & 1][i >> 1] = MFMA(a[i & 1], cat(m.bl[i >> 1], m.bh[i >> 1]), acc[i & 1][i >> 1]);
}
template <int ST> struct MC1 {
    static __device__ __forceinline__ void run(f32x4 (&acc)[2][6], MAF& a, MS1& s0, MS1& s1, const u32 (&ab)[2], const u32 (&b1)[4], const MBC& bc) {
        waitv<4 * (11 - ST < MBL - 1 ? 11 - ST : MBL - 1)>();
        SB(); barw(); SB();
#pragma unroll
        for (int i = 0; i < 8; ++i) {
            if constexpr (ST > 0) m_mma1(acc, a[ST - 1][1], s1, i);
            if (i < 5) m_read1<ST>(a, s0, 0, i, ab, b1);
            if constexpr (ST + MBL < 12) { if (i % 4 == MBP) m_bdma<ST + MBL>(bc, i / 4); }
            SB();
        }
#pragma unroll
        for (int i = 0; i < 8; ++i) {
            m_mma1(acc, a[ST][0], s0, i);
            if (i < 5) m_read1<ST>(a, s1, 1, i, ab, b1);
            if constexpr (ST + MBL < 12) { if (i % 4 == MBP) m_bdma<ST + MBL>(bc, 2 + i / 4); }
            SB();
        }
        if constexpr (ST < 11) MC1<ST + 1>::run(acc, a, s0, s1, ab, b1, bc);
    }
};
template <int ST> __device__ __forceinline__ void m_bpro(const MBC& bc) {
#pragma unroll
    for (int i = 0; i < 4; ++i) m_bdma<ST>(bc, i);
    if constexpr (ST + 1 < MBL) m_bpro<ST + 1>(bc);
}
static_assert(MBL <= 4, "slot reuse");
__device__ __forceinline__ void m_read2(MAF& a, MS2& r, int J, int i, const u32 (&ab)[2], const u32 (&b2)[2]) {
    const int hs = i / 3, j = i % 3, st = 2 * J + (hs >> 1);
    if (j == 0) {
        if (!mkeep(st)) {
            const u32 t = adr(ab[hs & 1], st * 8192);
            a[st][hs & 1][0] = ldsr128(t);
            a[st][hs & 1][1] = ldsr128(t + 2048);
        }
    } else {
        const u32 t = adr(b2[j - 1], mb2slot(st) + (hs & 1) * 4096);
        r.bl[hs][j - 1] = ldstr(t);
        r.bh[hs][j - 1] = ldstr(t + 512);
    }
}
template <bool DOR>
__device__ __forceinline__ void m_int2(f32x4 (&acc)[2][6], MAF& a, const MS2& m, MS2& r, int J, const u32 (&ab)[2], const u32 (&b2)[2]) {
#pragma unroll
    for (int i = 0; i < 16; ++i) {
        const int hs = i >> 2, n = (i >> 1) & 1, h = i & 1;
        acc[h][4 + n] = MFMA(a[2 * J - 2 + (hs >> 1)][hs & 1][h], cat(m.bl[hs][n], m.bh[hs][n]), acc[h][4 + n]);
        if (DOR && i < 12) m_read2(a, r, J, i, ab, b2);
        SB();
    }
}

__device__ __forceinline__ void m_cons(const Params& p, int wave, int lane, int et, int gt, int half, Stamps& stp) {
    const u32 base = (u32)(uintptr_t)smem;
    const int q = lane >> 4, c = lane & 15;
    const int kb = half * 768, sg = half ^ (et & 1);
    const rsrc_t rw = mk(p.ws, WS_BYTES);
    MBC bc;
    {
        const int t = wave * 64 + lane;
        u32x2 sv, zv, ep;
        const rsrc_t rs = mk(p.sc, F_ * 2), rz = mk(p.ze, F_ * 2);
        asm volatile("buffer_load_dwordx2 %0, %1, %2, 0 offen" : "=v"(sv) : "v"((kb + 4 * t) * 2), "s"(rs) : "memory");
        asm volatile("buffer_load_dwordx2 %0, %1, %2, 0 offen" : "=v"(zv) : "v"((kb + 4 * t) * 2), "s"(rz) : "memory");
        gld_ho2(ep, EP_OFF, rw, 0);
        bc.rv = mk(p.V, F_ * F_ * 2);
        bc.voff = (kb + 4 * wave + q) * (F_ * 2) + gt * 384 + (sg ? 0 : 128) + (((c >> 1) ^ (q | ((wave >> 1) << 2))) << 5) + (c & 1) * 16;
        bc.wb = base + wave * 1024;
        bc.rot = mrot(et, gt);
        m_bpro<0>(bc);
        asm volatile("s_waitcnt vmcnt(%3)" : "+v"(sv), "+v"(zv), "+v"(ep) : "n"(4 * MBL) : "memory");
        if (t < 192) {
            f32x4 s4, z4;
            s4[0] = __uint_as_float(sv[0] << 16); s4[1] = __uint_as_float(sv[0] & 0xffff0000u);
            s4[2] = __uint_as_float(sv[1] << 16); s4[3] = __uint_as_float(sv[1] & 0xffff0000u);
            z4[0] = -__uint_as_float(zv[0] << 16) * s4[0]; z4[1] = -__uint_as_float(zv[0] & 0xffff0000u) * s4[1];
            z4[2] = -__uint_as_float(zv[1] << 16) * s4[2]; z4[3] = -__uint_as_float(zv[1] & 0xffff0000u) * s4[3];
            *(LAS f32x4*)(base + MS_OFF + t * 16) = s4;
            *(LAS f32x4*)(base + MZ_OFF + t * 16) = z4;
        } else if (t == 192) {
            put_ctl((u64)ep[0] | ((u64)ep[1] << 32));
        }
    }
    barw();
    STAMP(1);
    const int qq = c >> 2, pp = c & 3;
    const int rp = wave & 1, cg = wave >> 1;
    u32 ab[2], b1[4], b2[2];
    ab[0] = base + (32 * rp + c) * 128 + ((q ^ ((c >> 1) & 7)) << 4);
    ab[1] = ab[0] ^ 64;
#pragma unroll
    for (int n = 0; n < 4; ++n) {
        const int t = n < 3 ? (sg ? 0 : 2) + 3 * cg + n : (sg ? 6 : 0) + cg;
        b1[n] = base + (8 * q + qq) * 256 + ((t ^ (qq | ((q & 1) << 2))) << 5) + 8 * pp;
    }
#pragma unroll
    for (int n = 0; n < 2; ++n) b2[n] = base + (8 * q + qq) * 128 + (((2 * cg + n) ^ ((qq >> 1) | ((q & 1) << 1))) << 5) + 8 * pp;
    f32x4 acc[2][6] = {};
    const u64 tag = get_tag();
    const u32 tl = (u32)tag, th = (u32)(tag >> 32);
    const int tile = et * 8 + gt;
    MAF af;
    MS2 t0, t1;
    u32x4 cn;
#define CNRD() do { cn = *(volatile LAS u32x4*)(base + CTL_OFF + 32); SB(); } while (0)
#define CNCK(g) do { if (__builtin_amdgcn_readfirstlane(cn[g]) < 4) await4(32 + 4 * (g)); asm volatile("" ::: "memory"); SB(); } while (0)
    {
        MS1 s0, s1;
        MC1<0>::run(acc, af, s0, s1, ab, b1, bc);
        STAMP(10);
        CNRD();
#pragma unroll
        for (int i = 0; i < 8; ++i) { m_mma1(acc, af[11][1], s1, i); SB(); }
    }
#pragma unroll
    for (int n = 0; n < 3; ++n)
#pragma unroll
        for (int h = 0; h < 2; ++h) {
            u32x4 v;
            v[0] = pk(acc[h][n][0], acc[h][n][1]); v[1] = tl;
            v[2] = pk(acc[h][n][2], acc[h][n][3]); v[3] = th;
            __builtin_amdgcn_raw_buffer_store_b128(v, rw, lane * 16, EX_OFF + ((((tile * 2 + (sg ^ 1)) * 2 + rp) * 6 + 3 * cg + n) * 2 + h) * 1024, 16);
        }
    STAMP(16);
    SB(); CNCK(0);
    STAMP(11);
#pragma unroll
    for (int i = 0; i < 12; ++i) { m_read2(af, t0, 0, i, ab, b2); SB(); }
    u32x4 ex[3][2];
    u32 exo[3];
#pragma unroll
    for (int n = 0; n < 3; ++n) exo[n] = n == 0 ? (sg ? 0 : 4) + cg : (sg ? 2 : 0) + 2 * cg + n - 1;
#define EXLD() do { _Pragma("unroll") for (int n = 0; n < 3; ++n) _Pragma("unroll") for (int h = 0; h < 2; ++h) \
        gld_ho(ex[n][h], lane * 16, rw, EX_OFF + ((((tile * 2 + sg) * 2 + rp) * 6 + exo[n]) * 2 + h) * 1024); SB(); } while (0)
    SB(); CNRD();
    m_int2<true>(acc, af, t0, t1, 1, ab, b2);
    CNCK(1);
    m_int2<true>(acc, af, t1, t0, 2, ab, b2);
    CNRD();
    m_int2<true>(acc, af, t0, t1, 3, ab, b2);
    CNCK(2); CNRD();
    if (XLD == 3) EXLD();
    m_int2<true>(acc, af, t1, t0, 4, ab, b2);
    CNCK(3);
    if (XLD == 4) EXLD();
    m_int2<true>(acc, af, t0, t1, 5, ab, b2);
    if (XLD == 5) EXLD();
    m_int2<false>(acc, af, t1, t0, 6, ab, b2);
    if (XLD == 6) EXLD();
    STAMP(13);
#pragma unroll 1
    for (u32 spins = 0; spins < (1u << 16); ++spins) {
        asm volatile("s_waitcnt vmcnt(0)" : "+v"(ex[0][0]), "+v"(ex[0][1]), "+v"(ex[1][0]), "+v"(ex[1][1]), "+v"(ex[2][0]), "+v"(ex[2][1]) :: "memory");
        bool ok = true;
#pragma unroll
        for (int n = 0; n < 3; ++n)
#pragma unroll
            for (int h = 0; h < 2; ++h) ok = ok && ex[n][h][1] == tl && ex[n][h][3] == th;
        if (__all(ok)) break;
#pragma unroll
        for (int n = 0; n < 3; ++n)
#pragma unroll
            for (int h = 0; h < 2; ++h) gld_ho(ex[n][h], lane * 16, rw, EX_OFF + ((((tile * 2 + sg) * 2 + rp) * 6 + exo[n]) * 2 + h) * 1024);
    }
    STAMP(14 + 4);
#pragma unroll
    for (int n = 0; n < 3; ++n) {
        u32x4 v;
#pragma unroll
        for (int h = 0; h < 2; ++h) {
            v[2 * h] = pk(acc[h][3 + n][0] + __uint_as_float(ex[n][h][0] << 16), acc[h][3 + n][1] + __uint_as_float(ex[n][h][0] & 0xffff0000u));
            v[2 * h + 1] = pk(acc[h][3 + n][2] + __uint_as_float(ex[n][h][2] << 16), acc[h][3 + n][3] + __uint_as_float(ex[n][h][2] & 0xffff0000u));
        }
        __builtin_amdgcn_raw_buffer_store_b128(v, rw, lane * 16, MF_OFF + (((2 * gt + sg) * 20 + 2 * et + rp) * 6 + exo[n]) * 1024, 16);
    }
}

constexpr u32 xslot(int st) { return st % 4 == 1 ? TOP_OFF : st % 4 == 0 ? 0 : (st % 4 - 1) * 32768; }
#define XROT 1
struct XCtx { u32 vU[2], vX[2], ldsb; rsrc_t ru, rx; int rot; };
template <int ST> struct XP {
    static __device__ __forceinline__ void issue(const XCtx& c) {
        int k = ST;
        if (XROT) { k = ST + c.rot; k = k >= 10 ? k - 10 : k; }
#pragma unroll
        for (int j = 0; j < 4; ++j) {
            glds(c.ldsb + xslot(ST) + 16384 + j * 1024, c.vX[j & 1], c.rx, k * 128 + j * 8 * 1280);
            glds(c.ldsb + xslot(ST) + j * 1024, c.vU[j >> 1], c.ru, k * 64 * 1280 + j * 4 * 1280);
        }
    }
    static __device__ __forceinline__ void pro(const XCtx& c) {
        issue(c);
        if constexpr (ST + 1 < XA) XP<ST + 1>::pro(c);
    }
    static __device__ __forceinline__ void run(const XCtx& c, Stamps& stp) {
        constexpr int REM = (9 - ST) < (XA - 1) ? (9 - ST) : (XA - 1);
        waitv<REM * 8>();
        barn();
        STAMP(2 + ST);
        if constexpr (ST + XA < 10) XP<ST + XA>::issue(c);
        if constexpr (ST + 1 < 10) XP<ST + 1>::run(c, stp);
    }
};
static_assert(XA <= 3, "slot reuse");

struct XSet { s16x4 al[4], ah[4]; bf16x8 b[4]; };
template <bool DOM, bool DOR>
__device__ __forceinline__ void x_phase(f32x4 (&acc)[4][4], const XSet& m, XSet& r, const u32 (&ab)[4], u32 bb, u32 aoff, u32 boff) {
#pragma unroll
    for (int i = 0; i < 16; ++i) {
        if (DOM) acc[i / 4][i % 4] = MFMA(cat(m.al[i / 4], m.ah[i / 4]), m.b[i % 4], acc[i / 4][i % 4]);
        if (DOR) {
            if (i == 0) r.al[0] = ldstr(ab[0] + aoff);
            if (i == 1) r.ah[0] = ldstr(ab[0] + aoff + 1024);
            if (i >= 2 && i < 6) r.b[i - 2] = ldsr128(bb + boff + (i - 2) * 2048);
            if (i >= 6 && i < 12 && !(i & 1)) r.al[(i - 4) / 2] = ldstr(ab[(i - 4) / 2] + aoff);
            if (i >= 6 && i < 12 && (i & 1)) r.ah[(i - 4) / 2] = ldstr(ab[(i - 4) / 2] + aoff + 1024);
        }
        SB();
    }
}

__device__ __forceinline__ void x_prod(const Params& p, int pw, int lane, int sb, int eblk, Stamps& stp) {
    const u32 base = (u32)(uintptr_t)smem;
    const int q = lane >> 4, c = lane & 15;
    XCtx cx;
    cx.ru = mk(p.U, D_ * D_ * 2); cx.rx = mk(p.x, S_ * D_ * 2);
#pragma unroll
    for (int h = 0; h < 2; ++h) {
        cx.vU[h] = (16 * pw + q) * 1280 + eblk * 256 + (((c >> 1) ^ (q | (h << 2))) << 5) + (c & 1) * 16;
        cx.vX[h] = (sb * 128 + 32 * pw + (lane >> 3)) * 1280 + (((lane & 7) ^ ((4 * h + q) & 7)) << 4);
    }
    cx.ldsb = base + pw * 4096;
    cx.rot = XROT == 1 ? (2 * eblk + 5 * (sb & 1)) % 10 : XROT == 2 ? 2 * eblk : (2 * eblk + (sb & 1)) % 10;
    XP<0>::pro(cx);
    STAMP(1);
    XP<0>::run(cx, stp);
}

__device__ __forceinline__ void x_cons(const Params& p, int wave, int lane, int sb, int eblk, Stamps& stp) {
    const u32 base = (u32)(uintptr_t)smem;
    const int q = lane >> 4, c = lane & 15;
    const int we = wave & 1, ws = wave >> 1;
    const int qq = c >> 2, pp = c & 3;
    const int sw = qq | ((q & 1) << 2);
    u64 ep = 0;
    if (wave == 0 && lane == 0) ep = ld_epoch(p);
    u32 ab[4], bb[2];
#pragma unroll
    for (int t = 0; t < 4; ++t) ab[t] = base + (8 * q + qq) * 256 + (((4 * we + t) ^ sw) << 5) + 8 * pp;
    bb[0] = base + 16384 + (64 * ws + c) * 128 + ((q ^ ((c >> 1) & 7)) << 4);
    bb[1] = bb[0] ^ 64;
    f32x4 acc[4][4] = {};
    XSet s0, s1;
#pragma unroll
    for (int st = 0; st < 10; ++st) {
        if (st == 2 && wave == 0 && lane == 0) put_ctl(ep);
        SB(); if (XA > 2) barw(); else barn(); SB();
        if (st == 0) x_phase<false, true>(acc, s1, s0, ab, bb[0], xslot(st), xslot(st));
        else x_phase<true, true>(acc, s1, s0, ab, bb[0], xslot(st), xslot(st));
        x_phase<true, true>(acc, s0, s1, ab, bb[1], xslot(st) + 8192, xslot(st));
    }
    x_phase<true, false>(acc, s1, s0, ab, 0, 0, 0);
    const rsrc_t rw = mk(p.ws, WS_BYTES);
#pragma unroll
    for (int ebl = 0; ebl < 2; ++ebl)
#pragma unroll
        for (int u = 0; u < 4; ++u) {
            u32x4 v;
            v[0] = pk(acc[2 * ebl][u][0], acc[2 * ebl][u][1]);
            v[1] = pk(acc[2 * ebl][u][2], acc[2 * ebl][u][3]);
            v[2] = pk(acc[2 * ebl + 1][u][0], acc[2 * ebl + 1][u][1]);
            v[3] = pk(acc[2 * ebl + 1][u][2], acc[2 * ebl + 1][u][3]);
            const u32 off = XF_OFF + ((sb * 20 + 4 * eblk + 2 * we + ebl) * 8 + 4 * ws + u) * 1024;
            __builtin_amdgcn_raw_buffer_store_b128(v, rw, lane * 16, off, 16);
        }
}

constexpr u32 YC = 1088;
__device__ __forceinline__ void glds_ho(u32 m0v, u32 voff, rsrc_t r, u32 soff) {
    asm volatile("s_mov_b32 m0, %0\n\ts_nop 0\n\tbuffer_load_dwordx4 %1, %2, %3 offen sc1 lds" ::"s"(m0v), "v"(voff), "s"(r), "s"(soff) : "memory");
}
__device__ __forceinline__ void y_mark(u32 bits) {
    __hip_atomic_fetch_or((LAS u32*)((u32)(uintptr_t)smem + CTL_OFF + 16), bits, __ATOMIC_RELAXED, __HIP_MEMORY_SCOPE_WORKGROUP);
}
__device__ __forceinline__ void y_load(const Params& p, int pw, int lane, int gb, u64 tag, bool updater, Stamps& stp) {
    const u32 base = (u32)(uintptr_t)smem;
    const rsrc_t rw = mk(p.ws, WS_BYTES);
    const u32 voff = MF_OFF + gb * 120 * 1024 + lane * 16;
    const u64* fl = (const u64*)((const char*)p.ws + FL_OFF);
    await4(8);
    u32 pend = pw < 2 ? 7u : 3u, infl = 0;
    const bool act = lane < 3 && ((pend >> lane) & 1);
    const u64* f = fl + (gb * 10 + pw + 4 * lane) * FLS;
#pragma unroll 1
    for (u32 spins = 0; spins < (1u << 16); ++spins) {
        u64 v = 0;
        if (act) v = __hip_atomic_load(f, __ATOMIC_RELAXED, __HIP_MEMORY_SCOPE_AGENT);
        const u32 nw = (u32)__ballot(v == tag) & pend;
        if (infl) { if (lane == 0) y_mark(infl << pw); infl = 0; }
        if (nw) {
#pragma unroll 1
            for (int k = 0; k < 3; ++k)
                if ((nw >> k) & 1) {
                    const u32 et = pw + 4 * k;
#pragma unroll
                    for (int j = 0; j < 12; ++j) {
                        glds_ho(base + (et * 12 + j) * YC, voff, rw, (et * 12 + j) * 1024);
                        if (j == YH - 1 && infl) { waitv<YH>(); if (lane == 0) y_mark(infl << pw); }
                    }
                    infl = 1u << (4 * k);
                }
            pend &= ~nw;
            if (!pend) break;
        } else {
            __builtin_amdgcn_s_sleep(PSL);
        }
    }
    STAMP(19);
    waitv<0>();
    if (lane == 0) y_mark(infl << pw);
    STAMP(29);
    if (updater && pw == 3) {
#pragma unroll 1
        for (u32 spins = 0; spins < (1u << 16); ++spins) {
            u64 v[4];
#pragma unroll
            for (int k = 0; k < 4; ++k) v[k] = __hip_atomic_load(fl + (k * 64 + lane) * FLS, __ATOMIC_RELAXED, __HIP_MEMORY_SCOPE_AGENT);
            if (__all(v[0] == tag && v[1] == tag && v[2] == tag && v[3] == tag)) break;
            __builtin_amdgcn_s_sleep(8);
        }
        if (lane == 0) __hip_atomic_store((u64*)((char*)p.ws + EP_OFF), tag, __ATOMIC_RELAXED, __HIP_MEMORY_SCOPE_AGENT);
    }
}

__device__ __forceinline__ void y_need(u32& have, int et) {
    if (!((have >> et) & 1)) {
        const LAS u32* w = (const LAS u32*)((u32)(uintptr_t)smem + CTL_OFF + 16);
        for (u32 spins = 0; spins < (1u << 22); ++spins) {
            have = __builtin_amdgcn_readfirstlane(__hip_atomic_load(w, __ATOMIC_RELAXED, __HIP_MEMORY_SCOPE_WORKGROUP));
            if ((have >> et) & 1) break;
            __builtin_amdgcn_s_sleep(1);
        }
    }
    asm volatile("" ::: "memory");
}

template <int V> struct IC { static constexpr int v = V; };
template <int A, int B, class F> __device__ __forceinline__ void static_for(F&& f) {
    if constexpr (A < B) { f(IC<A>{}); static_for<A + 1, B>(f); }
}
#define YS 7
constexpr int YP = YS < 10 ? (10 - YS) * 4 : 1;
constexpr int yord(int f) { return f < 12 * YS ? f : (2 * YS + ((f - 12 * YS) % YP) / 2) * 6 + 2 * ((f - 12 * YS) / YP) + (f & 1); }
constexpr bool yfirst(int f) { return f < 12 * YS ? f % 12 == 0 : f < 12 * YS + YP && (f - 12 * YS) % 4 == 0; }
__device__ __forceinline__ void y_mma(const Params& p, int w, int lane, int sb, int gb, u64 tag, Stamps& stp) {
    const u32 base = (u32)(uintptr_t)smem;
    const int q = lane >> 4, c = lane & 15;
    const rsrc_t rw = mk(p.ws, WS_BYTES);
    const u64* fl = (const u64*)((const char*)p.ws + FL_OFF);
    bf16x8 xa[20][2];
    bool thr = true;
    wait_ready(w == 0, fl + (160 + sb * 5 + lane) * FLS, lane < 5, tag, 20);
    STAMP(18);
#pragma unroll
    for (int eb = 0; eb < 20; ++eb) {
#pragma unroll
        for (int u = 0; u < 2; ++u)
            xa[eb][u] = __builtin_bit_cast(bf16x8, __builtin_amdgcn_raw_buffer_load_b128(rw, XF_OFF + (sb * 160 + 2 * w) * 1024 + lane * 16, (eb * 8 + u) * 1024, 16));
        if (eb % XTD == 0 && thr) thr = __builtin_amdgcn_readfirstlane(*(volatile LAS u32*)(base + CTL_OFF + 8)) < 4;
        if (2 * eb + 2 > XTH && thr) waitv<XTH>();
    }
    STAMP(19);
    const u32 bb0 = base + (c >> 3) * YC + q * 256 + ((c >> 2) & 1) * 128 + (c & 3) * 16, bb1 = bb0 + 60 * YC;
#define YADDR(f) (((f) < 60 ? bb0 : bb1) + (((f) % 60) / 2) * 2 * YC + ((f) & 1) * 64)
    f32x4 acc[2][6] = {};
    bf16x8 b[YR];
    u32 have = 0;
    const rsrc_t ry = mk(p.y, S_ * F_ * 2);
    const u32 yo = ((sb * 128 + 32 * w + c) * F_ + gb * 96 + 8 * q) * 2;
    auto ystore = [&](int h) __attribute__((always_inline)) {
#pragma unroll
        for (int u = 0; u < 2; ++u) {
            u32x4 v;
            v[0] = pk(acc[u][2 * h][0], acc[u][2 * h][1]);
            v[1] = pk(acc[u][2 * h][2], acc[u][2 * h][3]);
            v[2] = pk(acc[u][2 * h + 1][0], acc[u][2 * h + 1][1]);
            v[3] = pk(acc[u][2 * h + 1][2], acc[u][2 * h + 1][3]);
            __builtin_amdgcn_raw_buffer_store_b128(v, ry, yo, 16 * u * F_ * 2 + h * 64, YAUX);
        }
        SB();
    };
    SB(); y_need(have, 0); SB();
    STAMP(20);
#pragma unroll
    for (int f = 0; f < YR; ++f) {
        if (f && yfirst(f)) { y_need(have, yord(f) / 12); SB(); }
        b[f] = ldsr128(YADDR(yord(f)));
    }
    SB();
    static_for<0, 120>([&](auto fc) __attribute__((always_inline)) {
        constexpr int f = decltype(fc)::v, eb = yord(f) / 6, n = yord(f) % 6, g = yord(f + YR);
        acc[0][n] = MFMA(b[f % YR], xa[eb][0], acc[0][n]);
        acc[1][n] = MFMA(b[f % YR], xa[eb][1], acc[1][n]);
        SB();
        if constexpr (f + YR < 120) {
            if constexpr (yfirst(f + YR)) { y_need(have, g / 12); STAMP(20 + g / 12); SB(); }
            b[f % YR] = ldsr128(YADDR(g));
            SB();
        }
        if constexpr (YS < 10 && f == 12 * YS + YP + 1) ystore(0);
        if constexpr (YS < 10 && f == 12 * YS + 2 * YP + 1) ystore(1);
    });
    STAMP(30);
    if (YS == 10) { ystore(0); ystore(1); }
    ystore(2);
    asm volatile(".Lqk_end:");
}

__global__ __launch_bounds__(512, 2) void quip_kernel(Params p) {
    const int b = blockIdx.x, xcd = b & 7, i = b >> 3;
    const int lane = threadIdx.x & 63;
    const int wave = __builtin_amdgcn_readfirstlane(threadIdx.x >> 6);
    Stamps stp;
    u32 cpf = 0;
    if (wave == CPFW) {
        u64 pc; u32 len;
        asm volatile("s_getpc_b64 %0\n.Lqk_start:\n\ts_mov_b32 %1, .Lqk_end-.Lqk_start" : "=s"(pc), "=s"(len));
        const rsrc_t rc = mk((const void*)pc, len);
        asm volatile("buffer_load_dword %0, %1, %2, 0 offen" : "=v"(cpf) : "v"(((i % CPFN) * 64 + lane) * 128), "s"(rc) : "memory");
    }
    const int sb2 = 8 * (xcd & 1) + (i & 7), gb2 = 4 * (xcd >> 1) + (i >> 3);
    const int half = xcd & 1, et = 5 * ((xcd >> 1) & 1) + i % 5, gt = 4 * (xcd >> 2) + i / 5;
    const int j = i - 20, sb = 2 * xcd + j / 5, eblk = j % 5;
    if (wave >= 4) {
        if (i < 20) m_prod(p, wave - 4, lane, et, gt, half, stp);
        else if (i < 30) x_prod(p, wave - 4, lane, sb, eblk, stp);
        else barn();
        STAMP(15);
        y_mma(p, wave - 4, lane, sb2, gb2, get_tag(), stp);
    } else {
        int fidx;
        if (i < 20) { m_cons(p, wave, lane, et, gt, half, stp); fidx = (2 * gt + (half ^ (et & 1))) * 10 + et; }
        else if (i < 30) { x_cons(p, wave, lane, sb, eblk, stp); fidx = 160 + sb * 5 + eblk; }
        else {
            if (wave == 0 && lane == 0) put_ctl(ld_epoch(p));
            barw();
            fidx = b;
        }
        STAMP(15);
        const u64 tag = get_tag();
        publish(p, lane, fidx, tag);
        asm volatile("" :: "v"(cpf));
        STAMP(17);
        y_load(p, wave, lane, gb2, tag, b == 255, stp);
    }
}

extern "C" void kernel_launch(void* const* d_in, const int* in_sizes, int n_in, void* d_out, int out_size, void* d_ws,
                              size_t ws_size, hipStream_t stream) {
    Params p;
    p.x = d_in[0]; p.qw = d_in[1]; p.sc = d_in[2]; p.ze = d_in[3]; p.U = d_in[4]; p.V = d_in[5];
    p.y = d_out; p.ws = d_ws;
    quip_kernel<<<256, 512, 0, stream>>>(p);
}
```
